```python
import torch

def _softmax(x, axis=-1):
    return torch.softmax(x, dim=axis)
def _gelu(x):
    return torch.nn.functional.gelu(x, approximate="tanh")
def _silu(x):
    return torch.nn.functional.silu(x)
def _sigmoid(x):
    return torch.sigmoid(x)
def _layernorm(x, axis=-1, eps=1e-5):
    m = x.mean(dim=axis, keepdim=True)
    v = x.var(dim=axis, keepdim=True, unbiased=False)
    return (x - m) / torch.sqrt(v + eps)
def _scatter_add(tgt, idx, src, axis=0):
    return tgt.clone().index_add_(axis, idx, src)
def _segment_sum(src, seg, n):
    out = torch.zeros((n,) + src.shape[1:], dtype=src.dtype)
    return out.index_add_(0, seg, src)
def _pad(x, pw):
    flat = tuple(v for pair in reversed(pw) for v in pair)
    return torch.nn.functional.pad(x, flat)

kernel_name = 'parallel_sum__mha_block__glu_gated_bilinear__prompt'

def setup_inputs():

    _w = (lambda a: (lambda t: t.to(torch.bfloat16) if t.is_floating_point() else t)(torch.as_tensor(a)))
    def _A_setup():
        B, S, D, H = 1, 2048, 640, 32
        return {
            "x": _w((lambda *s: torch.randn(*s, dtype=torch.bfloat16) * 0.05)(B, S, D)),
            "wqkv": _w((lambda *s: torch.randn(*s, dtype=torch.bfloat16) * 0.05)(D, 3 * D)),
            "wo": _w((lambda *s: torch.randn(*s, dtype=torch.bfloat16) * 0.05)(D, D)),
        }
    _A_in = _A_setup()

    def _B_setup():
        B, S, D, F = 1, 2048, 640, 1728
        return {
            "x": _w((lambda *s: torch.randn(*s, dtype=torch.bfloat16) * 0.05)(B, S, D)),
            "w_gate": _w((lambda *s: torch.randn(*s, dtype=torch.bfloat16) * 0.8)(D, F) * (D ** -0.5)),
            "w_up": _w((lambda *s: torch.randn(*s, dtype=torch.bfloat16) * 0.8)(D, F) * (D ** -0.5)),
            "w_down": _w((lambda *s: torch.randn(*s, dtype=torch.bfloat16) * 0.8)(F, D) * (F ** -0.5)),
        }
    _B_in = _B_setup()

    return {
        "A_x": _A_in["x"],
        "A_wqkv": _A_in["wqkv"],
        "A_wo": _A_in["wo"],
        "B_x": _B_in["x"],
        "B_w_gate": _B_in["w_gate"],
        "B_w_up": _B_in["w_up"],
        "B_w_down": _B_in["w_down"],
    }

def reference(A_x, A_wqkv, A_wo, B_x, B_w_gate, B_w_up, B_w_down):
    def _A_ref(x, wqkv, wo):
        B, S, D = x.shape
        H = 32
        Dh = D // H
        qkv = (lambda x, *d: x.permute(*d))((x @ wqkv).reshape(B, S, 3, H, Dh), 2, 0, 3, 1, 4)
        q, k, v = qkv[0], qkv[1], qkv[2]
        scores = torch.einsum("bhqd,bhkd->bhqk", q, k) * (Dh ** -0.5)
        p = _softmax(scores, axis=-1)
        o = torch.einsum("bhqk,bhkd->bhqd", p, v)
        return (lambda x, *d: x.permute(*d))(o, 0, 2, 1, 3).reshape(B, S, D) @ wo

    def _B_ref(x, w_gate, w_up, w_down):
        g = torch.einsum("bsd,df->bsf", x, w_gate)
        u = torch.einsum("bsd,df->bsf", x, w_up)
        h = g * u
        return torch.einsum("bsf,fd->bsd", h, w_down)

    x = A_x
    xn = _layernorm(x, axis=-1)
    return x + _A_ref(xn, A_wqkv, A_wo) + _B_ref(xn, B_w_gate, B_w_up, B_w_down)
```

```cpp
#include <hip/hip_runtime.h>
#include <hip/hip_bf16.h>

typedef unsigned short u16;
typedef __attribute__((ext_vector_type(8))) __bf16 bf16x8;
typedef __attribute__((ext_vector_type(2))) __bf16 bf16x2;
typedef __attribute__((ext_vector_type(4))) short s16x4;
typedef __attribute__((ext_vector_type(16))) float f32x16;
typedef __attribute__((ext_vector_type(4))) float f32x4;
typedef __attribute__((ext_vector_type(2))) float f32x2;
typedef __attribute__((ext_vector_type(4))) unsigned u32x4;
typedef __attribute__((ext_vector_type(2))) unsigned u32x2;
#define LAS __attribute__((address_space(3)))
typedef LAS const char* lds_cptr;

constexpr int S_ = 2048, D_ = 640, F_ = 1728, N1_ = 1920;
constexpr int QT = 1280;
constexpr int KVT = 2624;
constexpr int KVH = 64 * KVT;
constexpr int KS3 = 74;
constexpr size_t WS_XN = 0;
constexpr size_t WS_Q = WS_XN + (size_t)64 * 40 * 1024;
constexpr size_t WS_KV = WS_Q + (size_t)32 * 64 * QT;
constexpr size_t WS_A3 = WS_KV + (size_t)32 * KVH + 4096;
constexpr size_t WS_W3 = WS_A3 + (size_t)32 * KS3 * 4096;

#define MFMA32(a, b, c) __builtin_amdgcn_mfma_f32_32x32x16_bf16(a, b, c, 0, 0, 0)
#define MFMA32X8(a, b, c) __builtin_amdgcn_mfma_f32_32x32x8bf16_1k(a, b, c, 0, 0, 0)
#define MFMA16(a, b, c) __builtin_amdgcn_mfma_f32_16x16x32_bf16(a, b, c, 0, 0, 0)
#define SBAR() __builtin_amdgcn_sched_barrier(0)
#define WAIT_BAR(N) asm volatile("s_waitcnt vmcnt(" #N ") lgkmcnt(0)\n\ts_barrier" ::: "memory")
#define LGKM_BAR() asm volatile("s_waitcnt lgkmcnt(0)\n\ts_barrier" ::: "memory")

#define STAMP(k, i)
__device__ __forceinline__ void glds16(const void* g, unsigned lds_base) {
  unsigned sv;
  asm volatile("s_mov_b32 %0, m0\n\ts_mov_b32 m0, %2\n\ts_nop 0\n\tglobal_load_lds_dwordx4 %1, off\n\ts_mov_b32 m0, %0"
               : "=&s"(sv) : "v"(g), "s"(lds_base) : "memory");
}
__device__ __forceinline__ unsigned pk2(float lo, float hi) {
  f32x2 v = {lo, hi};
  return __builtin_bit_cast(unsigned, __builtin_convertvector(v, bf16x2));
}
__device__ __forceinline__ float bfr(float v) { return (float)(__bf16)v; }
__device__ __forceinline__ float bflo(unsigned u) { return __uint_as_float(u << 16); }
__device__ __forceinline__ float bfhi(unsigned u) { return __uint_as_float(u & 0xffff0000u); }
__device__ __forceinline__ bf16x8 lds128(lds_cptr p) { return *(const LAS bf16x8*)p; }
__device__ __forceinline__ s16x4 lds64(lds_cptr p) { return *(const LAS s16x4*)p; }
__device__ __forceinline__ bf16x8 ldstr2(lds_cptr p, int second) {
  s16x4 a = __builtin_amdgcn_ds_read_tr16_b64_v4i16((LAS s16x4*)p);
  s16x4 b = __builtin_amdgcn_ds_read_tr16_b64_v4i16((LAS s16x4*)(p + second));
  return __builtin_bit_cast(bf16x8, __builtin_shufflevector(a, b, 0, 1, 2, 3, 4, 5, 6, 7));
}
__device__ __forceinline__ float dot8(bf16x8 a, bf16x8 b, float c) {
  u32x4 x = __builtin_bit_cast(u32x4, a), y = __builtin_bit_cast(u32x4, b);
#pragma unroll
  for (int i = 0; i < 4; i++) {
    const unsigned xi = x[i], yi = y[i];
    c = __builtin_amdgcn_fdot2_f32_bf16(__builtin_bit_cast(bf16x2, xi), __builtin_bit_cast(bf16x2, yi), c, false);
  }
  return c;
}
__device__ __forceinline__ float xhalf_sum(float v) {
  auto r = __builtin_amdgcn_permlane32_swap(__float_as_uint(v), __float_as_uint(v), false, false);
  return __uint_as_float(r[0]) + __uint_as_float(r[1]);
}
__device__ __forceinline__ float xhalf_max(float v) {
  auto r = __builtin_amdgcn_permlane32_swap(__float_as_uint(v), __float_as_uint(v), false, false);
  return fmaxf(__uint_as_float(r[0]), __uint_as_float(r[1]));
}
__device__ __forceinline__ void ln_stats(float sum, float ssq, float& mean_b, float& std_b) {
  float mean = sum * (1.0f / D_);
  float var = fmaxf(ssq * (1.0f / D_) - mean * mean, 0.0f);
  mean_b = bfr(mean);
  std_b = bfr(sqrtf(bfr(bfr(var) + 1e-5f)));
}

constexpr int K1_SLOT = 32768, K1_NSLOT = 4, K1_LDS = K1_SLOT * K1_NSLOT;

template <bool SWAP>
__device__ __forceinline__ void k1_compute(char* smem, int lane, int wm, int wn, f32x16 (&acc)[2][2], float& rsum, float& rssq, float& csum) {
  const int r = lane & 31, hh = lane >> 5;
  const int q = (lane & 15) >> 2, p = lane & 3, g2 = (lane >> 4) & 1;
  lds_cptr base = (lds_cptr)smem;
  int xo[4];
#pragma unroll
  for (int ks = 0; ks < 4; ks++) xo[ks] = (wm * 64 + wn * 32 + r) * 128 + (((2 * ks + hh) ^ ((r >> 1) & 7)) << 4);
  int wo[2];
#pragma unroll
  for (int b = 0; b < 2; b++) wo[b] = 16384 + (8 * hh + q) * 256 + (((wn * 2 + (b ^ wm)) ^ q) << 6) + g2 * 32 + p * 8;
  bf16x8 ones;
#pragma unroll
  for (int i = 0; i < 8; i++) ones[i] = (__bf16)1.0f;
  for (int s = 0; s < 10; s++) {
    LGKM_BAR();
    lds_cptr sb = base + (s & 3) * K1_SLOT;
#pragma unroll
    for (int ks = 0; ks < 4; ks++) {
      bf16x8 xf[2], wf[2];
      xf[0] = lds128(sb + xo[ks]);
      xf[1] = lds128(sb + (xo[ks] ^ 4096));
      wf[0] = ldstr2(sb + wo[0] + ks * 4096, 1024);
      wf[1] = ldstr2(sb + wo[1] + ks * 4096, 1024);
#pragma unroll
      for (int bn = 0; bn < 2; bn++)
#pragma unroll
        for (int bs = 0; bs < 2; bs++)
          acc[bn][bs] = SWAP ? MFMA32(wf[bn], xf[bs], acc[bn][bs]) : MFMA32(xf[bs], wf[bn], acc[bn][bs]);
      rsum = dot8(xf[0], ones, rsum);
      rssq = dot8(xf[0], xf[0], rssq);
      csum = dot8(wf[0], ones, csum);
    }
  }
}

__global__ __launch_bounds__(512) void k1_qkv(const u16* __restrict__ x, const u16* __restrict__ wqkv, char* __restrict__ ws) {
  __shared__ __attribute__((aligned(16))) char smem[K1_LDS];
  const int tid = threadIdx.x, lane = tid & 63;
  const int wave = __builtin_amdgcn_readfirstlane(tid >> 6);
  const int xcd = blockIdx.x & 7, o = blockIdx.x >> 3;
  STAMP(0, 0);
  if (o >= 30) {
    const int rb = xcd * 2 + (o - 30), j = lane & 15;
    u32x4 v[4][5];
#pragma unroll
    for (int ps = 0; ps < 4; ps++) {
      const int row = rb * 128 + wave * 16 + ps * 4 + (lane >> 4);
#pragma unroll
      for (int i = 0; i < 5; i++) v[ps][i] = *(const u32x4*)(x + (size_t)row * D_ + (j + 16 * i) * 8);
    }
#pragma unroll
    for (int ps = 0; ps < 4; ps++) {
      const int row = rb * 128 + wave * 16 + ps * 4 + (lane >> 4);
      float sum = 0.f;
#pragma unroll
      for (int i = 0; i < 5; i++)
#pragma unroll
        for (int e = 0; e < 4; e++) sum += bflo(v[ps][i][e]) + bfhi(v[ps][i][e]);
#pragma unroll
      for (int m = 1; m < 16; m <<= 1) sum += __shfl_xor(sum, m);
      const float mean = sum * (1.0f / D_);
      float ssq = 0.f;
#pragma unroll
      for (int i = 0; i < 5; i++)
#pragma unroll
        for (int e = 0; e < 4; e++) {
          float a = bflo(v[ps][i][e]) - mean, b = bfhi(v[ps][i][e]) - mean;
          ssq += a * a + b * b;
        }
#pragma unroll
      for (int m = 1; m < 16; m <<= 1) ssq += __shfl_xor(ssq, m);
      const float mean_b = bfr(mean);
      const float rstd = 1.0f / bfr(sqrtf(bfr(bfr(ssq * (1.0f / D_)) + 1e-5f)));
#pragma unroll
      for (int i = 0; i < 5; i++) {
        u32x4 w;
#pragma unroll
        for (int e = 0; e < 4; e++) {
          unsigned d = pk2(bflo(v[ps][i][e]) - mean_b, bfhi(v[ps][i][e]) - mean_b);
          w[e] = pk2(bflo(d) * rstd, bfhi(d) * rstd);
        }
        const int c = j + 16 * i;
        *(u32x4*)(ws + WS_XN + ((size_t)(row >> 5) * 40 + (c >> 1)) * 1024 + ((c & 1) * 32 + (row & 31)) * 16) = w;
      }
    }
    STAMP(0, 3);
    return;
  }
  const int rb = xcd * 2 + (o & 1), cb = o >> 1;
  const unsigned lds0 = (unsigned)(uintptr_t)smem;
  float* sst = (float*)smem;
  if (wave >= 4) {
    const int lw = wave - 4;
    const u16* xs[4]; const u16* wsrc[4];
#pragma unroll
    for (int i = 0; i < 4; i++) {
      const int rr = (lw * 4 + i) * 8 + (lane >> 3);
      xs[i] = x + (size_t)(rb * 128 + rr) * D_ + (((lane & 7) ^ ((rr >> 1) & 7)) << 3);
      const int kk = (lw * 4 + i) * 4 + (lane >> 4);
      wsrc[i] = wqkv + (size_t)kk * N1_ + cb * 128 + (((lane & 15) ^ ((kk & 3) << 2)) << 3);
    }
    int kst = (rb + cb) % 10;
    auto issue = [&](int s) {
      const unsigned dst = lds0 + (s & 3) * K1_SLOT + lw * 4096;
#pragma unroll
      for (int i = 0; i < 4; i++) glds16(xs[i] + kst * 64, dst + i * 1024);
#pragma unroll
      for (int i = 0; i < 4; i++) glds16(wsrc[i] + (size_t)kst * 64 * N1_, dst + 16384 + i * 1024);
      kst = (kst == 9) ? 0 : kst + 1;
    };
    issue(0); issue(1); issue(2);
    STAMP(0, 4);
    for (int s = 0; s < 7; s++) { WAIT_BAR(16); if (s == 0) STAMP(0, 5); issue(s + 3); }
    WAIT_BAR(16); WAIT_BAR(8); WAIT_BAR(0);
    LGKM_BAR(); LGKM_BAR();
    return;
  }
  const int wm = wave >> 1, wn = wave & 1, r = lane & 31, hh = lane >> 5;
  f32x16 acc[2][2] = {};
  float rsum = 0.f, rssq = 0.f, csum = 0.f;
  if (cb < 10) k1_compute<true>(smem, lane, wm, wn, acc, rsum, rssq, csum);
  else k1_compute<false>(smem, lane, wm, wn, acc, rsum, rssq, csum);
  STAMP(0, 2);
  rsum = xhalf_sum(rsum); rssq = xhalf_sum(rssq); csum = xhalf_sum(csum);
  float mean_b, std_b;
  ln_stats(rsum, rssq, mean_b, std_b);
  LGKM_BAR();
  if (hh == 0) {
    const float rs = 1.0f / std_b;
    sst[wm * 64 + wn * 32 + r] = rs;
    sst[128 + wm * 64 + wn * 32 + r] = -mean_b * rs;
    sst[256 + wn * 64 + wm * 32 + r] = csum;
  }
  LGKM_BAR();
  if (cb < 10) {
    const bool isq = cb < 5;
    const float sc = isq ? 0.22360679774997896f * 1.4426950408889634f : 1.0f;
    char* img = isq ? ws + WS_Q : ws + WS_KV;
    const int hs = isq ? 64 * QT : KVH, ts = isq ? QT : KVT, p2 = isq ? 1024 : 2368;
    const int cq0 = ((isq ? cb : cb - 5) * 128 + wn * 64) / 4 + hh;
#pragma unroll
    for (int bs = 0; bs < 2; bs++) {
      const int sl = wm * 64 + (bs ^ wn) * 32 + r;
      const float rs = sst[sl] * sc, mr = sst[128 + sl] * sc;
      char* tb = img + (size_t)(rb * 4 + wm * 2 + (bs ^ wn)) * ts;
#pragma unroll
      for (int bn = 0; bn < 2; bn++)
#pragma unroll
        for (int i = 0; i < 4; i++) {
          const f32x4 cs = *(const f32x4*)(sst + 256 + wn * 64 + (bn ^ wm) * 32 + 8 * i + 4 * hh);
          u32x2 w;
          w[0] = pk2(acc[bn][bs][4 * i] * rs + mr * cs[0], acc[bn][bs][4 * i + 1] * rs + mr * cs[1]);
          w[1] = pk2(acc[bn][bs][4 * i + 2] * rs + mr * cs[2], acc[bn][bs][4 * i + 3] * rs + mr * cs[3]);
          const int c = cq0 + (bn ^ wm) * 8 + 2 * i, head = (c * 52429) >> 18, dq = c - head * 5;
          const int off = (dq < 4) ? ((dq >> 1) * 32 + r) * 16 + (dq & 1) * 8 : p2 + r * 8;
          *(u32x2*)(tb + (size_t)head * hs + off) = w;
        }
    }
  } else {
#pragma unroll
    for (int bn = 0; bn < 2; bn++) {
      const int nl = wn * 64 + (bn ^ wm) * 32 + r, nf = (cb - 10) * 128 + nl;
      const int head = (nf * 52429) >> 20, d = nf - head * 20;
      const float cs = sst[256 + nl];
#pragma unroll
      for (int bs = 0; bs < 2; bs++) {
        char* tb = ws + WS_KV + (size_t)head * KVH + (size_t)(rb * 4 + wm * 2 + (bs ^ wn)) * KVT + 1024 + hh * 336 + d * 16;
#pragma unroll
        for (int m = 0; m < 2; m++) {
          u32x4 w;
#pragma unroll
          for (int a = 0; a < 2; a++) {
            const int i = 2 * m + a, sl = wm * 64 + (bs ^ wn) * 32 + 8 * i + 4 * hh;
            const f32x4 rs = *(const f32x4*)(sst + sl), mr = *(const f32x4*)(sst + 128 + sl);
            w[2 * a] = pk2(acc[bn][bs][4 * i] * rs[0] + mr[0] * cs, acc[bn][bs][4 * i + 1] * rs[1] + mr[1] * cs);
            w[2 * a + 1] = pk2(acc[bn][bs][4 * i + 2] * rs[2] + mr[2] * cs, acc[bn][bs][4 * i + 3] * rs[3] + mr[3] * cs);
          }
          *(u32x4*)(tb + m * 672) = w;
          if (d == 19) *(u32x4*)(tb + m * 672 + 16) = u32x4{0x3f803f80u, 0x3f803f80u, 0x3f803f80u, 0x3f803f80u};
        }
      }
    }
  }
  STAMP(0, 3);
}

constexpr int AT_BUF = 21504;
constexpr int G_SLOT = 24576;
constexpr int K2_LDS = 3 * G_SLOT;

struct KFrag { bf16x8 k1; s16x4 k2; };
struct VFrag { bf16x8 v0, v1; };

__device__ __forceinline__ f32x16 qk_tile(const KFrag& k, bf16x8 q1, s16x4 q2) {
  f32x16 z = {};
  f32x16 s = MFMA32(k.k1, q1, z);
  return MFMA32X8(k.k2, q2, s);
}
__device__ __forceinline__ bf16x8 exp_pack(const f32x16& s, int m) {
  u32x4 w;
#pragma unroll
  for (int i = 0; i < 4; i++) w[i] = pk2(__builtin_amdgcn_exp2f(s[8 * m + 2 * i]), __builtin_amdgcn_exp2f(s[8 * m + 2 * i + 1]));
  return __builtin_bit_cast(bf16x8, w);
}
__device__ __forceinline__ float max16(const f32x16& s) {
  float a = fmaxf(s[0], s[1]);
#pragma unroll
  for (int i = 2; i < 16; i += 2) a = fmaxf(fmaxf(a, s[i]), s[i + 1]);
  return a;
}

__device__ __forceinline__ void attn_role(char* smem, char* __restrict__ ws, int head, int qb, int tid, int wave) {
  const int lane = tid & 63, r = lane & 31, hh = lane >> 5;
  const unsigned lds0 = (unsigned)(uintptr_t)smem;
  const char* kvsrc = ws + WS_KV + (size_t)head * KVH + wave * 1024 + lane * 16;
  auto dma = [&](int c) {
    const char* src = kvsrc + (size_t)c * (8 * KVT);
    const unsigned dst = lds0 + (c & 1) * AT_BUF + wave * 1024;
    glds16(src, dst);
    glds16(src + 8192, dst + 8192);
    if (wave < 5) glds16(src + 16384, dst + 16384);
  };
  dma(0);
  const char* qp = ws + WS_Q + ((size_t)head * 64 + qb * 8 + wave) * QT;
  const bf16x8 q1 = *(const bf16x8*)(qp + lane * 16);
  u32x2 q2u = *(const u32x2*)(qp + 1024 + r * 8);
  if (hh) q2u = u32x2{0u, 0u};
  dma(1);
  const int k1o = lane * 16, k2o = hh ? 1024 + 320 : 2368 + r * 8, vo = 1024 + (hh * 21 + r) * 16;
  lds_cptr lb = (lds_cptr)smem;
  LAS int* flag = (LAS int*)(lb + 2 * AT_BUF + 512);
  auto ldk = [&](lds_cptr t) { KFrag k; k.k1 = lds128(t + k1o); k.k2 = lds64(t + k2o); return k; };
  auto ldv = [&](lds_cptr t) { VFrag v; v.v0 = lds128(t + vo); v.v1 = lds128(t + vo + 672); return v; };
  auto set_ref = [&](float m) {
    const float m0 = bfr(m), m1 = bfr(m - m0), m2 = bfr(m - m0 - m1);
    if (hh) q2u = u32x2{pk2(-m0, -m1), pk2(-m2, 0.f)};
  };
  WAIT_BAR(0);
  {
    KFrag k = ldk(lb);
    set_ref(xhalf_max(max16(qk_tile(k, q1, __builtin_bit_cast(s16x4, q2u)))));
  }
  f32x16 o;
  STAMP(1, 1);
  for (int attempt = 0;; attempt++) {
    const s16x4 q2 = __builtin_bit_cast(s16x4, q2u);
    o = f32x16{};
    KFrag ka = ldk(lb);
    f32x16 sa = qk_tile(ka, q1, q2);
    ka = ldk(lb + KVT);
    VFrag va = ldv(lb);
    for (int c = 0; c < 8; c++) {
      lds_cptr cur = lb + (c & 1) * AT_BUF, nxt = lb + ((c + 1) & 1) * AT_BUF;
#pragma unroll
      for (int t = 0; t < 8; t++) {
        KFrag kb; VFrag vb;
        if (t < 6) { kb = ldk(cur + (t + 2) * KVT); vb = ldv(cur + (t + 1) * KVT); }
        else if (t == 6) {
          vb = ldv(cur + 7 * KVT);
          WAIT_BAR(0);
          if (c < 6) dma(c + 2);
          kb = ldk(nxt);
        } else { kb = ldk(nxt + KVT); vb = ldv(nxt); }
        f32x16 sb = qk_tile(ka, q1, q2);
        o = MFMA32(va.v0, exp_pack(sa, 0), o);
        o = MFMA32(va.v1, exp_pack(sa, 1), o);
        sa = sb; ka = kb; va = vb;
      }
    }
    if (attempt == 1) break;
    bool ok = true;
#pragma unroll
    for (int i = 0; i < 12; i++) ok = ok && (fabsf(o[i]) < 1e30f || (hh && i > 8));
    LGKM_BAR();
    if (tid == 0) *flag = 0;
    LGKM_BAR();
    if (!ok) *flag = 1;
    LGKM_BAR();
    if (*flag == 0) break;
    if (hh) q2u = u32x2{0u, 0u};
    const s16x4 qz = __builtin_bit_cast(s16x4, q2u);
    float mx = -INFINITY;
    for (int c = 0; c < 8; c++) {
      LGKM_BAR();
      dma(c);
      WAIT_BAR(0);
      lds_cptr cur = lb + (c & 1) * AT_BUF;
      for (int t = 0; t < 8; t++) { KFrag k = ldk(cur + t * KVT); mx = fmaxf(mx, max16(qk_tile(k, q1, qz))); }
    }
    set_ref(xhalf_max(mx));
    LGKM_BAR();
    dma(0); dma(1);
    WAIT_BAR(0);
  }
  STAMP(1, 2);
  auto sw = __builtin_amdgcn_permlane32_swap(__float_as_uint(o[8]), __float_as_uint(o[8]), false, false);
  const float inv = 1.0f / __uint_as_float(sw[1]);
  const int s = qb * 256 + wave * 32 + r;
  char* ab = ws + WS_A3 + ((size_t)(s >> 6) * KS3 * 4 + ((s >> 4) & 3)) * 1024 + (s & 15) * 16;
#pragma unroll
  for (int i = 0; i < 3; i++) {
    if (i == 2 && hh) break;
    const int c = head * 5 + 2 * i + hh, g = 2 * ((c >> 2) & 1) + (c & 1), a = (c >> 1) & 1;
    u32x2 w = {pk2(o[4 * i] * inv, o[4 * i + 1] * inv), pk2(o[4 * i + 2] * inv, o[4 * i + 3] * inv)};
    *(u32x2*)(ab + (c >> 3) * 4096 + g * 256 + a * 8) = w;
  }
}

__device__ __forceinline__ void glu_role(char* smem, const u16* __restrict__ wg, const u16* __restrict__ wu, char* __restrict__ ws,
                                         int R, int fb, int tid, int wave) {
  const int lane = tid & 63, r = lane & 31, hh = lane >> 5;
  const unsigned lds0 = (unsigned)(uintptr_t)smem;
  const char* xsrc = ws + WS_XN + (size_t)(R * 8 + wave) * 40 * 1024 + lane * 16;
  const int wr = lane >> 3;
  const u16* wsrc = ((wave >> 2) ? wu : wg) + (size_t)((wave & 3) * 8 + wr) * F_ + fb * 64 + (((lane & 7) ^ (((wr >> 1) & 1) << 2)) << 3);
  int kst = (R * 5 + fb * 3) % 20;
  int slot_w = 0;
  auto issue = [&]() {
    const unsigned dst = lds0 + slot_w;
    glds16(xsrc + kst * 2048, dst + wave * 2048);
    glds16(xsrc + kst * 2048 + 1024, dst + wave * 2048 + 1024);
    glds16(wsrc + (size_t)kst * 32 * F_, dst + 16384 + wave * 1024);
    kst = (kst == 19) ? 0 : kst + 1;
    slot_w = (slot_w == 2 * G_SLOT) ? 0 : slot_w + G_SLOT;
  };
  issue(); issue();
  const int wm = wave >> 1, wf = wave & 1;
  const int q = (lane & 15) >> 2, p = lane & 3, g2 = (lane >> 4) & 1;
  const int xo = wm * 4096 + lane * 16;
  const int wo = 16384 + (8 * hh + q) * 128 + ((wf ^ ((q >> 1) & 1)) << 6) + g2 * 32 + p * 8;
  f32x16 ag[2] = {}, au[2] = {};
  int slot_r = 0;
  for (int s = 0; s < 20; s++) {
    if (s < 19) WAIT_BAR(3); else WAIT_BAR(0);
    if (s < 18) issue();
    lds_cptr sb = (lds_cptr)smem + slot_r;
#pragma unroll
    for (int j = 0; j < 2; j++) {
      const bf16x8 x0 = lds128(sb + xo + j * 1024), x1 = lds128(sb + xo + 2048 + j * 1024);
      const bf16x8 gf = ldstr2(sb + wo + j * 2048, 512), uf = ldstr2(sb + wo + 4096 + j * 2048, 512);
      ag[0] = MFMA32(gf, x0, ag[0]); ag[1] = MFMA32(gf, x1, ag[1]);
      au[0] = MFMA32(uf, x0, au[0]); au[1] = MFMA32(uf, x1, au[1]);
    }
    slot_r = (slot_r == 2 * G_SLOT) ? 0 : slot_r + G_SLOT;
  }
  STAMP(1, 2);
#pragma unroll
  for (int bs = 0; bs < 2; bs++) {
    const int s = R * 256 + wm * 64 + bs * 32 + r;
    char* ab = ws + WS_A3 + (((size_t)(s >> 6) * KS3 + 20 + fb * 2 + wf) * 4 + ((s >> 4) & 3)) * 1024 + ((s & 15) + 16 * hh) * 16;
#pragma unroll
    for (int m = 0; m < 2; m++) {
      u32x4 w;
#pragma unroll
      for (int e = 0; e < 4; e++) {
        const unsigned gp = pk2(ag[bs][8 * m + 2 * e], ag[bs][8 * m + 2 * e + 1]), up = pk2(au[bs][8 * m + 2 * e], au[bs][8 * m + 2 * e + 1]);
        w[e] = pk2(bflo(gp) * bflo(up), bfhi(gp) * bfhi(up));
      }
      *(u32x4*)(ab + m * 512) = w;
    }
  }
}

__device__ __forceinline__ void retile_role(const u16* __restrict__ wo, const u16* __restrict__ wd, char* __restrict__ ws, int idx, int tid, int wave) {
  const int lane = tid & 63, g = lane >> 4;
  for (int f = idx * 8 + wave; f < 8 * KS3 * 5; f += 320) {
    const int nb = f / (KS3 * 5), rem = f - nb * (KS3 * 5), ks = rem / 5, t = rem - ks * 5;
    const int k0 = 32 * ks + 16 * (g >> 1) + 4 * (g & 1), n = nb * 80 + t * 16 + (lane & 15);
    const u16* src = (ks < 20) ? wo + (size_t)k0 * D_ + n : wd + (size_t)(k0 - 640) * D_ + n;
    u32x4 w;
#pragma unroll
    for (int e = 0; e < 4; e++) {
      const int kk = 8 * (e >> 1) + 2 * (e & 1);
      w[e] = (unsigned)src[kk * D_] | ((unsigned)src[(kk + 1) * D_] << 16);
    }
    *(u32x4*)(ws + WS_W3 + (size_t)f * 1024 + lane * 16) = w;
  }
}

__global__ __launch_bounds__(512, 4) void k2_attn_glu(const u16* __restrict__ wo, const u16* __restrict__ wg, const u16* __restrict__ wu,
                                                      const u16* __restrict__ wd, char* __restrict__ ws) {
  __shared__ __attribute__((aligned(16))) char smem[K2_LDS];
  const int tid = threadIdx.x;
  const int wave = __builtin_amdgcn_readfirstlane(tid >> 6);
  const int b = blockIdx.x, xcd = b & 7, o = (b >> 3) & 31;
  STAMP(1, 0);
  if (b < 256) {
    __builtin_amdgcn_s_setprio(2);
    attn_role(smem, ws, xcd * 4 + (o >> 3), o & 7, tid, wave);
  } else if (o < 27) {
    glu_role(smem, wg, wu, ws, xcd, o, tid, wave);
  } else {
    retile_role(wo, wd, ws, (o - 27) * 8 + xcd, tid, wave);
  }
  STAMP(1, 3);
}

struct Frag3 { bf16x8 a[4], w[5]; };
__global__ __launch_bounds__(512) void k3_out(const u16* __restrict__ x, const char* __restrict__ ws, u16* __restrict__ out) {
  __shared__ __attribute__((aligned(16))) char smem[8 * 20480];
  const int tid = threadIdx.x, lane = tid & 63;
  const int wave = __builtin_amdgcn_readfirstlane(tid >> 6);
  const int xcd = blockIdx.x & 7, o = blockIdx.x >> 3, rb = xcd * 4 + (o >> 3), nb = o & 7;
  const char* ap = ws + WS_A3 + (size_t)rb * KS3 * 4096 + lane * 16;
  const char* wp = ws + WS_W3 + (size_t)nb * KS3 * 5120 + lane * 16;
  auto load = [&](Frag3& f, int ks) {
#pragma unroll
    for (int i = 0; i < 4; i++) f.a[i] = *(const bf16x8*)(ap + (size_t)ks * 4096 + i * 1024);
#pragma unroll
    for (int i = 0; i < 5; i++) f.w[i] = *(const bf16x8*)(wp + (size_t)ks * 5120 + i * 1024);
  };
  f32x4 acc[5][4] = {};
  auto comp = [&](const Frag3& f) {
#pragma unroll
    for (int t = 0; t < 5; t++)
#pragma unroll
      for (int i = 0; i < 4; i++) acc[t][i] = MFMA16(f.w[t], f.a[i], acc[t][i]);
  };
  STAMP(2, 0);
  Frag3 f[3];
  const int w0 = (wave + o) & 7;
  load(f[0], w0); load(f[1], w0 + 8);
#pragma unroll
  for (int it = 0; it < 9; it++) {
    if (it + 2 < 9) load(f[(it + 2) % 3], w0 + 8 * (it + 2));
    else if (it + 2 == 9 && w0 < 2) load(f[(it + 2) % 3], w0 + 72);
    comp(f[it % 3]);
  }
  if (w0 < 2) comp(f[0]);
  STAMP(2, 2);
  f32x4* red = (f32x4*)smem;
#pragma unroll
  for (int t = 0; t < 5; t++)
#pragma unroll
    for (int i = 0; i < 4; i++) red[wave * 1280 + (t * 4 + i) * 64 + lane] = acc[t][i];
  __syncthreads();
#pragma unroll
  for (int u = 0; u < 3; u++) {
    const int idx = tid + u * 512;
    if (idx < 1280) {
      f32x4 v = red[idx];
#pragma unroll
      for (int w = 1; w < 8; w++) v += red[w * 1280 + idx];
      const int ti = idx >> 6, l = idx & 63;
      const size_t eo = (size_t)(rb * 64 + (ti & 3) * 16 + (l & 15)) * D_ + nb * 80 + (ti >> 2) * 16 + (l >> 4) * 4;
      const u32x2 xv = *(const u32x2*)(x + eo);
      u32x2 w = {pk2(v[0] + bflo(xv[0]), v[1] + bfhi(xv[0])), pk2(v[2] + bflo(xv[1]), v[3] + bfhi(xv[1]))};
      *(u32x2*)(out + eo) = w;
    }
  }
  STAMP(2, 3);
}

extern "C" void kernel_launch(void* const* d_in, const int* in_sizes, int n_in, void* d_out, int out_size, void* d_ws, size_t ws_size,
                              hipStream_t stream) {
  const u16* x = (const u16*)d_in[0];
  const u16* wqkv = (const u16*)d_in[1];
  const u16* wo = (const u16*)d_in[2];
  const u16* wg = (const u16*)d_in[4];
  const u16* wu = (const u16*)d_in[5];
  const u16* wd = (const u16*)d_in[6];
  char* ws = (char*)d_ws;
  k1_qkv<<<256, 512, 0, stream>>>(x, wqkv, ws);
  k2_attn_glu<<<512, 512, 0, stream>>>(wo, wg, wu, wd, ws);
  k3_out<<<256, 512, 0, stream>>>(x, ws, (u16*)d_out);
}
```

```cpp
#include <hip/hip_runtime.h>
#include <hip/hip_bf16.h>

typedef unsigned short u16;
typedef __attribute__((ext_vector_type(8))) __bf16 bf16x8;
typedef __attribute__((ext_vector_type(2))) __bf16 bf16x2;
typedef __attribute__((ext_vector_type(4))) short s16x4;
typedef __attribute__((ext_vector_type(8))) short s16x8;
typedef __attribute__((ext_vector_type(16))) float f32x16;
typedef __attribute__((ext_vector_type(4))) float f32x4;
typedef __attribute__((ext_vector_type(2))) float f32x2;
typedef __attribute__((ext_vector_type(4))) unsigned u32x4;
typedef __attribute__((ext_vector_type(2))) unsigned u32x2;
#define LAS __attribute__((address_space(3)))
typedef LAS const char* lds_cptr;

constexpr int S_ = 2048, D_ = 640, F_ = 1728, N1_ = 1920;
constexpr int QT = 1280;
constexpr int VB = 352;
constexpr int KVT = 1024 + 4 * VB;
constexpr int KVP = 2 * KVT + 512;
constexpr int KVH = 32 * KVP;
constexpr int KS3 = 74;
constexpr size_t WS_ST = 0;
constexpr size_t WS_Q = WS_ST + 16384;
constexpr size_t WS_KV = WS_Q + (size_t)32 * 64 * QT;
constexpr size_t WS_A3 = WS_KV + (size_t)32 * KVH;
constexpr size_t WS_W3 = WS_A3 + (size_t)32 * KS3 * 4096;
constexpr size_t WS_P3 = WS_W3 + (size_t)8 * KS3 * 5120;

#define MFMA32(a, b, c) __builtin_amdgcn_mfma_f32_32x32x16_bf16(a, b, c, 0, 0, 0)
#define MFMA32X8(a, b, c) __builtin_amdgcn_mfma_f32_32x32x8bf16_1k(a, b, c, 0, 0, 0)
#define MFMA16(a, b, c) __builtin_amdgcn_mfma_f32_16x16x32_bf16(a, b, c, 0, 0, 0)
#define SGB(m, n) __builtin_amdgcn_sched_group_barrier(m, n, 0)
#define ST16(p, v) (*(u32x4*)(p) = (v))
#define ST8(p, v) (*(u32x2*)(p) = (v))
#define SBAR() __builtin_amdgcn_sched_barrier(0)
#define PIN(x) asm volatile("" : "+v"(x))
#define WAIT_BAR(N) asm volatile("s_waitcnt vmcnt(" #N ") lgkmcnt(0)\n\ts_barrier" ::: "memory")
#define LGKM_BAR() asm volatile("s_waitcnt lgkmcnt(0)\n\ts_barrier" ::: "memory")

#define X_PH 1
#define X_LM 0
#define XL(b) if (!(X_LM & b))
#define PF_DECL
#define PF_ARG
#define PF_PASS
#define STAMP(k, i)
__device__ __forceinline__ void glds16(const void* g, unsigned lds_base) {
  asm volatile("s_mov_b32 m0, %1\n\ts_nop 0\n\tglobal_load_lds_dwordx4 %0, off" :: "v"(g), "s"(lds_base) : "memory", "m0");
}
__device__ __forceinline__ unsigned pk2(float lo, float hi) {
  f32x2 v = {lo, hi};
  return __builtin_bit_cast(unsigned, __builtin_convertvector(v, bf16x2));
}
__device__ __forceinline__ float bfr(float v) { return (float)(__bf16)v; }
__device__ __forceinline__ float bflo(unsigned u) { return __uint_as_float(u << 16); }
__device__ __forceinline__ float bfhi(unsigned u) { return __uint_as_float(u & 0xffff0000u); }
__device__ __forceinline__ bf16x8 lds128(lds_cptr p) { return *(const LAS bf16x8*)p; }
__device__ __forceinline__ s16x4 lds64(lds_cptr p) { return *(const LAS s16x4*)p; }
__device__ __forceinline__ bf16x8 ldstr2(lds_cptr p, int second) {
  s16x4 a = __builtin_amdgcn_ds_read_tr16_b64_v4i16((LAS s16x4*)p);
  s16x4 b = __builtin_amdgcn_ds_read_tr16_b64_v4i16((LAS s16x4*)(p + second));
  return __builtin_bit_cast(bf16x8, __builtin_shufflevector(a, b, 0, 1, 2, 3, 4, 5, 6, 7));
}
__device__ __forceinline__ float xhalf_max(float v) {
  auto r = __builtin_amdgcn_permlane32_swap(__float_as_uint(v), __float_as_uint(v), false, false);
  return fmaxf(__uint_as_float(r[0]), __uint_as_float(r[1]));
}
__device__ __forceinline__ void ln_stats(float sum, float ssq, float& mean_b, float& std_b) {
  float mean = sum * (1.0f / D_);
  float var = fmaxf(ssq * (1.0f / D_) - mean * mean, 0.0f);
  mean_b = bfr(mean);
  std_b = bfr(sqrtf(bfr(bfr(var) + 1e-5f)));
}

constexpr int XSLOT = 32768, WSLOT = 12288, K1_X0 = 4 * WSLOT, K1_LDS = 3 * XSLOT + 4 * WSLOT;

template <int MODE>
struct K1Core {
  static constexpr int NB = MODE == 2 ? 6 : 5;
  f32x4 acc[NB][4], cs[2], rs[2], sq[2];
  bf16x8 xf[2][4], wf[2][NB], sel;
  const u16 *xs0, *xs1, *wpa, *wpb;
  int wstep, xo, wo[5], wave;
  unsigned lds0;
  lds_cptr lb;
  template <int PAR> __device__ __forceinline__ void issue_x(int h, int xslot) {
    const int off = ((h + 5) >> 1) * 64 + (PAR ^ 1) * 128 * D_;
    const unsigned dst = lds0 + K1_X0 + xslot + (PAR ^ 1) * 16384 + wave * 2048;
    glds16(xs0 + off, dst); glds16(xs1 + off, dst + 1024);
  }
  template <int PAR, int WM> __device__ __forceinline__ void issue_w(int h) {
    glds16(wpa + (h + 4) * wstep, lds0 + (h & 3) * WSLOT + wave * 1024);
    if ((WM >> 1) == PAR) glds16(wpb + (h + 4) * wstep, lds0 + (h & 3) * WSLOT + (8 + (wave & 3)) * 1024);
  }
  template <int PAR> __device__ __forceinline__ void load(int wslot, int xslot) {
    lds_cptr xb = lb + K1_X0 + xslot + (xo ^ (PAR * 64)), xc = lb + K1_X0 + xslot + (xo ^ (PAR * 64) ^ 4096);
#pragma unroll
    for (int j = 0; j < NB; j++) {
      if (j < 4) xf[PAR][j] = lds128((j < 2 ? xb : xc) + (j & 1) * 2048);
      wf[PAR][j] = ldstr2(lb + wslot + (MODE == 2 ? wo[j % 3] + (j / 3) * 6144 : wo[j]), 256);
    }
  }
  template <int PAR, int WM> __device__ __forceinline__ void mma(int j0, int j1) {
#pragma unroll
    for (int j = j0; j < j1; j++) {
#pragma unroll
      for (int a = 0; a < 4; a++) acc[j][a] = MODE == 1 ? MFMA16(xf[PAR][a], wf[PAR][j], acc[j][a]) : MFMA16(wf[PAR][j], xf[PAR][a], acc[j][a]);
      if ((j & 3) == WM) cs[j >> 2] = MFMA16(sel, wf[PAR][j], cs[j >> 2]);
    }
  }
  template <int PAR, int WM> __device__ __forceinline__ void step(int h, int xslot_w, int xslot_next, int wslot_next) {
    if (h < 16) WAIT_BAR(7); else WAIT_BAR(0);
    if (WM < 2) { if (h < 15) issue_x<PAR>(h, xslot_w); if (X_PH == 1 && h < 16) issue_w<PAR, WM>(h); }
    asm volatile("" : "+v"(xf[PAR][0]), "+v"(xf[PAR][1]), "+v"(xf[PAR][2]), "+v"(xf[PAR][3]));
    SBAR();
    load<PAR ^ 1>(wslot_next, xslot_next);
    mma<PAR, WM>(0, 4);
    SGB(0x2, 2);
#pragma unroll
    for (int i = 0; i < 4 + 2 * NB; i++) { SGB(0x8, 1); SGB(0x2, 1); SGB(0x100, 1); }
    SBAR();
#pragma unroll
    for (int j = 0; j < 4; j++) asm volatile("" : "+v"(acc[j][0]), "+v"(acc[j][1]), "+v"(acc[j][2]), "+v"(acc[j][3]));
    asm volatile("" : "+v"(cs[0]), "+v"(xf[PAR][0]), "+v"(xf[PAR][1]), "+v"(xf[PAR][2]), "+v"(xf[PAR][3]));
    if (X_PH == 2 && h < 16) issue_w<PAR, WM>(h);
    SBAR();
    mma<PAR, WM>(4, NB);
#pragma unroll
    for (int a = 0; a < 2; a++) {
      rs[a] = MFMA16(sel, xf[PAR][a], rs[a]);
      if (MODE != 2) sq[a] = MFMA16(xf[PAR][a], xf[PAR][a], sq[a]);
    }
    SBAR();
#pragma unroll
    for (int j = 4; j < NB; j++) asm volatile("" : "+v"(acc[j][0]), "+v"(acc[j][1]), "+v"(acc[j][2]), "+v"(acc[j][3]));
    asm volatile("" : "+v"(cs[1]), "+v"(rs[0]), "+v"(rs[1]));
    if (MODE != 2) asm volatile("" : "+v"(sq[0]), "+v"(sq[1]));
    if (WM >= 2) { if (h < 15) issue_x<PAR>(h, xslot_w); if (X_PH == 1 && h < 16) issue_w<PAR, WM>(h); }
  }
  template <int WM> __device__ __forceinline__ void run(char* ws PF_ARG) {
    issue_x<1>(-5, 0); issue_x<0>(-4, 0); issue_w<0, WM>(-4);
    issue_x<1>(-3, XSLOT); issue_w<1, WM>(-3); issue_x<0>(-2, XSLOT); issue_w<0, WM>(-2);
#pragma unroll
    for (int j = 0; j < NB; j++) for (int a = 0; a < 4; a++) acc[j][a] = f32x4{};
    cs[0] = cs[1] = rs[0] = rs[1] = sq[0] = sq[1] = f32x4{};
    STAMP(0, 4);
    WAIT_BAR(7);
    STAMP(0, 5);
    load<0>(0, 0);
    issue_x<1>(-1, 2 * XSLOT); issue_w<1, WM>(-1);
    int s0 = 0, s1 = XSLOT, s2 = 2 * XSLOT;
    for (int h = 0; h < 20; h += 2) {
      const int wb = (h & 2) * WSLOT;
      step<0, WM>(h, s2, s0, wb + WSLOT);
      step<1, WM>(h + 1, s0, s1, wb ^ (2 * WSLOT));
      const int t = s0; s0 = s1; s1 = s2; s2 = t;
    }
  }
};

template <int MODE>
__device__ __forceinline__ void k1_tile(char* smem, const u16* __restrict__ x, const u16* __restrict__ w0, const u16* __restrict__ w1,
                                        char* __restrict__ ws, int rb, int ct, int tid, int wave PF_ARG) {
  constexpr int NB = K1Core<MODE>::NB, LD = MODE == 2 ? F_ : N1_;
  const int lane = tid & 63, i = lane & 15, g = lane >> 4, wm = wave >> 1, wn = wave & 1;
  K1Core<MODE> c;
  c.wave = wave; c.lds0 = (unsigned)(uintptr_t)smem; c.lb = (lds_cptr)smem; c.wstep = 32 * LD;
  c.xs0 = x + (size_t)(rb * 256 + wave * 16 + (lane >> 3)) * D_ + (((lane & 7) ^ g) << 3);
  c.xs1 = x + (size_t)(rb * 256 + wave * 16 + 8 + (lane >> 3)) * D_ + (((lane & 7) ^ (4 + g)) << 3);
  {
    const int kr = (wave & 1) * 16 + (lane >> 2), ch = ((lane & 3) ^ ((lane >> 5) << 1)) << 3;
    const int ua = wave >> 1, ub = 4 + ((wave & 3) >> 1);
    if (MODE == 2) {
      c.wpa = (ua < 3 ? w0 + ua * 32 : w1) + (size_t)kr * LD + ct * 96 + ch;
      c.wpb = w1 + (ub - 3) * 32 + (size_t)kr * LD + ct * 96 + ch;
    } else {
      c.wpa = w0 + ua * 32 + (size_t)kr * LD + ct * 160 + ch;
      c.wpb = w0 + 128 + (size_t)kr * LD + ct * 160 + ch;
    }
  }
  c.xo = (wm * 64 + wn * 32 + i) * 128 + ((g ^ (i >> 1)) << 4);
  {
    const int q = i >> 2, p = lane & 3;
#pragma unroll
    for (int j = 0; j < 5; j++) {
      const int Q = MODE == 0 ? 5 * p + j + 20 * wn : MODE == 1 ? 4 * j + p + 20 * wn : 4 * (wn ? (j == 2 ? 3 : 4 + j) : j) + p;
      c.wo[j] = (Q >> 3) * 2048 + (g >> 1) * 1024 + (8 * (g & 1) + q) * 64 + (((Q & 7) ^ (4 * (g & 1))) << 3);
    }
    const unsigned v = i == 0 ? 0x3f803f80u : 0u;
    c.sel = __builtin_bit_cast(bf16x8, u32x4{v, v, v, v});
  }
  if (wm == 0) c.template run<0>(ws PF_PASS);
  else if (wm == 1) c.template run<1>(ws PF_PASS);
  else if (wm == 2) c.template run<2>(ws PF_PASS);
  else c.template run<3>(ws PF_PASS);
  STAMP(0, 2);
  float* sc = (float*)smem;
  if (g == 0) {
#pragma unroll
    for (int a = 0; a < 2; a++) sc[wm * 64 + wn * 32 + a * 16 + i] = c.rs[a][0];
    sc[1792 + wn * 96 + wm * 16 + i] = c.cs[0][0];
    if (wm + 4 < NB) sc[1792 + wn * 96 + (wm + 4) * 16 + i] = c.cs[1][0];
  }
  if (MODE != 2 && g == (i >> 2)) {
#pragma unroll
    for (int a = 0; a < 2; a++) {
      const float s01 = (i & 1) ? c.sq[a][1] : c.sq[a][0], s23 = (i & 1) ? c.sq[a][3] : c.sq[a][2];
      sc[256 + wm * 64 + wn * 32 + a * 16 + i] = (i & 2) ? s23 : s01;
    }
  }
  LGKM_BAR();
  if (tid < 256) {
    if (MODE == 2) {
      sc[1536 + tid] = -bfr(sc[tid] * (1.0f / D_));
    } else {
      float mean_b, std_b;
      ln_stats(sc[tid], sc[256 + tid], mean_b, std_b);
      const float r = 1.0f / std_b;
      sc[1280 + tid] = r; sc[1536 + tid] = -mean_b * r;
      if (ct == 0) ((float*)(ws + WS_ST))[rb * 256 + tid] = r * r;
    }
  }
  LGKM_BAR();
  if (MODE == 0) {
    const bool isq = ct < 4;
    const float scl = isq ? 0.22360679774997896f * 1.4426950408889634f : 1.0f;
    const int head = (ct & 3) * 8 + wn * 4 + g;
    f32x4 cv[5];
#pragma unroll
    for (int j = 0; j < 5; j++) cv[j] = *(const f32x4*)(sc + 1792 + wn * 96 + j * 16 + 4 * g);
#pragma unroll
    for (int a = 0; a < 4; a++) {
      const int row = wm * 64 + ((a * 16) ^ (wn * 32)) + i, odd = (a >> 1) ^ wn;
      const float r = sc[1280 + row] * scl, m = sc[1536 + row] * scl;
      unsigned w[10];
#pragma unroll
      for (int j = 0; j < 5; j++) {
        w[2 * j] = pk2(c.acc[j][a][0] * r + m * cv[j][0], c.acc[j][a][1] * r + m * cv[j][1]);
        w[2 * j + 1] = pk2(c.acc[j][a][2] * r + m * cv[j][2], c.acc[j][a][3] * r + m * cv[j][3]);
      }
      const int rr = isq ? (a & 1) * 16 + i : 16 * (i >> 3) + 8 * (a & 1) + (i & 7);
      char* tb = isq ? ws + WS_Q + (size_t)(head * 64 + rb * 8 + wm * 2 + odd) * QT : ws + WS_KV + (size_t)head * KVH + (rb * 4 + wm) * KVP + odd * KVT;
      ST16(tb + rr * 16, (u32x4{w[0], w[1], w[2], w[3]}));
      ST16(tb + 512 + rr * 16, (u32x4{w[4], w[5], w[6], w[7]}));
      ST8(tb + (isq ? 1024 + rr * 8 : 2 * KVT - odd * (KVT - 8) + rr * 16), (u32x2{w[8], w[9]}));
    }
  } else if (MODE == 1) {
#pragma unroll
    for (int ap = 0; ap < 2; ap++) {
      const int odd = ap ^ wn, row = wm * 64 + odd * 32 + 4 * g;
      const f32x4 r0 = *(const f32x4*)(sc + 1280 + row), r1 = *(const f32x4*)(sc + 1296 + row);
      const f32x4 m0 = *(const f32x4*)(sc + 1536 + row), m1 = *(const f32x4*)(sc + 1552 + row);
#pragma unroll
      for (int j = 0; j < 5; j++) {
        const int n = 16 * j + i, hl = (n * 52429) >> 20, d = n - hl * 20;
        const float cv = sc[1792 + wn * 96 + n];
        const f32x4 u = c.acc[j][2 * ap] * r0 + m0 * cv, v = c.acc[j][2 * ap + 1] * r1 + m1 * cv;
        char* tb = ws + WS_KV + (size_t)((ct & 3) * 8 + wn * 4 + hl) * KVH + (rb * 4 + wm) * KVP + odd * KVT + 1024 + g * VB + d * 16;
        ST16(tb, (u32x4{pk2(u[0], u[1]), pk2(u[2], u[3]), pk2(v[0], v[1]), pk2(v[2], v[3])}));
        if (j > 0 && d >= 18) { const unsigned k = (d == 18) ? 0x3f803f80u : 0u; ST16(tb + 32, (u32x4{k, k, k, k})); }
      }
    }
  } else {
    f32x4 cv[6];
#pragma unroll
    for (int j = 0; j < 6; j++) cv[j] = *(const f32x4*)(sc + 1792 + wn * 96 + j * 16 + 4 * g);
#pragma unroll
    for (int a = 0; a < 4; a++) {
      const int sub = a ^ (2 * wn);
      const float m = sc[1536 + wm * 64 + sub * 16 + i];
      unsigned w[6];
#pragma unroll
      for (int j = 0; j < 3; j++)
#pragma unroll
        for (int e = 0; e < 2; e++) {
          const unsigned gp = pk2(c.acc[j][a][2 * e] + m * cv[j][2 * e], c.acc[j][a][2 * e + 1] + m * cv[j][2 * e + 1]);
          const unsigned up = pk2(c.acc[j + 3][a][2 * e] + m * cv[j + 3][2 * e], c.acc[j + 3][a][2 * e + 1] + m * cv[j + 3][2 * e + 1]);
          w[2 * j + e] = pk2(bflo(gp) * bflo(up), bfhi(gp) * bfhi(up));
        }
      char* ab = ws + WS_A3 + (((size_t)(rb * 4 + wm) * KS3 + 20 + ct * 3) * 4 + sub) * 1024 + lane * 16;
      ST16(ab + wn * 8192, (u32x4{w[0], w[1], w[2], w[3]}));
      ST8(ab + 4096 + wn * 8, (u32x2{w[4], w[5]}));
    }
  }
}

__device__ __forceinline__ void retile_role(char* smem, const u16* __restrict__ wo, const u16* __restrict__ wd, char* __restrict__ ws,
                                            int idx, int tid, int wave) {
  const int lane = tid & 63, g = lane >> 4, q = (lane & 15) >> 2, p = lane & 3;
  const unsigned lds0 = (unsigned)(uintptr_t)smem;
  for (int k0 = idx; k0 < KS3; k0 += 48) {
    LGKM_BAR();
    for (int u = 0; u < 3; u++) {
      const int ks = k0 + 16 * u;
      if (ks < KS3) {
        const char* src = (const char*)(ks < 20 ? wo + (size_t)ks * 32 * D_ : wd + (size_t)(ks - 20) * 32 * D_) + wave * 5120 + lane * 16;
#pragma unroll
        for (int e = 0; e < 5; e++) glds16(src + e * 1024, lds0 + u * 40960 + wave * 5120 + e * 1024);
      }
    }
    WAIT_BAR(0);
    for (int u = 0; u < 3; u++) {
      const int ks = k0 + 16 * u;
      if (ks < KS3) {
        const int row = ks < 20 ? 16 * (g >> 1) + 4 * (g & 1) + q : 4 * g + q, sec = (ks < 20 ? 8 : 16) * 1280;
        lds_cptr b = (lds_cptr)smem + u * 40960 + row * 1280 + wave * 160 + p * 8;
#pragma unroll
        for (int t = 0; t < 5; t++)
          ST16(ws + WS_W3 + (((size_t)wave * KS3 + ks) * 5 + t) * 1024 + lane * 16, __builtin_bit_cast(u32x4, ldstr2(b + t * 32, sec)));
      }
    }
  }
}

__global__ __launch_bounds__(512) void k1_proj(const u16* __restrict__ x, const u16* __restrict__ wqkv, const u16* __restrict__ wg,
                                               const u16* __restrict__ wu, const u16* __restrict__ wo, const u16* __restrict__ wd,
                                               char* __restrict__ ws) {
  __shared__ __attribute__((aligned(16))) char smem[K1_LDS];
  const int tid = threadIdx.x;
  const int wave = __builtin_amdgcn_readfirstlane(tid >> 6);
  const int xcd = blockIdx.x & 7, o = blockIdx.x >> 3, cg = xcd >> 1, t = o >> 2;
  const int rb = (xcd & 1) * 4 + (o & 3);
  PF_DECL; STAMP(0, 0);
  if (t < 3) {
    const int ct = cg * 3 + t;
    if (ct < 8) k1_tile<0>(smem, x, wqkv, wqkv, ws, rb, ct, tid, wave PF_PASS);
    else k1_tile<1>(smem, x, wqkv, wqkv, ws, rb, ct, tid, wave PF_PASS);
  } else if (t < 7 || !(cg & 1)) {
    k1_tile<2>(smem, x, wg, wu, ws, rb, (cg >> 1) * 9 + (cg & 1) * 5 + t - 3, tid, wave PF_PASS);
  } else {
    retile_role(smem, wo, wd, ws, (cg >> 1) * 8 + rb, tid, wave);
  }
  STAMP(0, 3);
}

constexpr int AT_BUF = 4 * KVP;
constexpr int K2_LDS = 3 * AT_BUF + 1024;

#define TOFF(t) (((t) >> 1) * KVP + ((t) & 1) * KVT)
struct KFrag { bf16x8 k1; s16x4 k2; };

__device__ __forceinline__ f32x16 qk_tile(const KFrag& k, bf16x8 q1, s16x4 q2) {
  f32x16 z = {};
  f32x16 s = MFMA32(k.k1, q1, z);
  return MFMA32X8(k.k2, q2, s);
}
__device__ __forceinline__ bf16x8 exp_pack(const f32x16& s, int m) {
  u32x4 w;
#pragma unroll
  for (int i = 0; i < 4; i++) w[i] = pk2(__builtin_amdgcn_exp2f(s[8 * m + 2 * i]), __builtin_amdgcn_exp2f(s[8 * m + 2 * i + 1]));
  return __builtin_bit_cast(bf16x8, w);
}
__device__ __forceinline__ float max16(const f32x16& s) {
  float a = fmaxf(s[0], s[1]);
#pragma unroll
  for (int i = 2; i < 16; i += 2) a = fmaxf(fmaxf(a, s[i]), s[i + 1]);
  return a;
}

#define AT_GROUP(MF, ACC, A, B, P, PJ, SJ) do { unsigned pa_, pb_; SBAR();                                                        \
    asm volatile(MF "\n\tv_cvt_pk_bf16_f32 %1, %3, %4\n\tv_cvt_pk_bf16_f32 %2, %5, %6\n\tv_exp_f32 %3, %9\n\tv_exp_f32 %4, %10\n\t"    \
                 "v_exp_f32 %5, %11\n\tv_exp_f32 %6, %12"                                                                          \
                 : ACC, "=&v"(pa_), "=&v"(pb_), "+v"(e[0]), "+v"(e[1]), "+v"(e[2]), "+v"(e[3])                                     \
                 : "v"(A), "v"(B), "v"(sa[SJ]), "v"(sa[SJ + 1]), "v"(sa[SJ + 2]), "v"(sa[SJ + 3]));                                \
    P[PJ] = pa_; P[PJ + 1] = pb_; SBAR(); } while (0)

__device__ __forceinline__ void attn_role(char* smem, char* __restrict__ ws, int head, int qb, int tid, int wave PF_ARG) {
  const int lane = tid & 63, r = lane & 31, hh = lane >> 5;
  const unsigned lds0 = (unsigned)(uintptr_t)smem;
  const char* kvsrc = ws + WS_KV + (size_t)head * KVH + wave * 1024 + lane * 16;
  auto dma = [&](int c, int buf) {
    const char* src = kvsrc + (size_t)c * AT_BUF;
    const unsigned dst = lds0 + buf + wave * 1024;
    glds16(src, dst);
    glds16(src + 8192, dst + 8192);
    if (wave < 5) glds16(src + 16384, dst + 16384);
  };
  dma(0, 0);
  const char* qp = ws + WS_Q + ((size_t)head * 64 + qb * 8 + wave) * QT;
  const bf16x8 q1 = *(const bf16x8*)(qp + lane * 16);
  u32x2 q2u = *(const u32x2*)(qp + 1024 + r * 8);
  if (hh) q2u = u32x2{0u, 0u};
  const int k1o = lane * 16, k2o = hh ? 1024 + 320 : 2 * KVT + r * 16, vo = 1024 + hh * VB + min(r, 21) * 16;
  lds_cptr lb = (lds_cptr)smem;
  LAS int* flag = (LAS int*)(lb + 3 * AT_BUF + 512);
  auto ldk = [&](int t) { KFrag k; k.k1 = lds128(lb + TOFF(t) + k1o); k.k2 = lds64(lb + (t >> 1) * KVP + (t & 1) * 8 + k2o); return k; };
  auto set_ref = [&](float m) {
    const float m0 = bfr(m), m1 = bfr(m - m0), m2 = bfr(m - m0 - m1);
    if (hh) q2u = u32x2{pk2(-m0, -m1), pk2(-m2, 0.f)};
  };
  WAIT_BAR(0);
  dma(1, AT_BUF);
  {
    KFrag k = ldk(0);
    set_ref(xhalf_max(max16(qk_tile(k, q1, __builtin_bit_cast(s16x4, q2u)))));
  }
  f32x16 o;
  STAMP(1, 1);
  for (int attempt = 0;; attempt++) {
    const s16x4 q2 = __builtin_bit_cast(s16x4, q2u);
    o = f32x16{};
    f32x16 sa;
    { KFrag k = ldk(0); sa = qk_tile(k, q1, q2); }
    bf16x8 k1 = lds128(lb + KVT + k1o), v1 = {}, v0;
    s16x8 k2p = __builtin_bit_cast(s16x8, lds128(lb + k2o));
    v0 = lds128(lb + vo);
    u32x4 p0 = {0u, 0u, 0u, 0u}, p1 = {0u, 0u, 0u, 0u};
    float e[4] = {0.f, 0.f, 0.f, 0.f};
    int cur = 0, nxt = AT_BUF;
    asm volatile("s_nop 15\n\ts_nop 7" : "+v"(sa), "+v"(o), "+v"(p1), "+v"(v1));
    for (int c = 0; c < 8; c++) {
      lds_cptr ck1 = lb + cur + k1o, ck2 = lb + cur + k2o, cv = lb + cur + vo;
      lds_cptr nk1 = lb + nxt + k1o, nk2 = lb + nxt + k2o, nv = lb + nxt + vo;
      const int nn = (nxt == 2 * AT_BUF) ? 0 : nxt + AT_BUF;
#pragma unroll
      for (int t = 0; t < 8; t++) {
        XL(8) if (t == 6) asm volatile("s_waitcnt vmcnt(0)\n\ts_barrier" ::: "memory");
        const int pc = (t + 2) & 7;
        const bool ld = pc < 3 && (pc < 2 ? c < 6 : (c > 0 && c < 7 && wave < 5));
        const char* src = kvsrc + (size_t)(t == 0 ? c + 1 : c + 2) * AT_BUF + pc * 8192;
        const unsigned dst = lds0 + (t == 0 ? nxt : nn) + wave * 1024 + pc * 8192;
        XL(8) if (ld && wave < 4) glds16(src, dst);
        f32x16 sb;
        const s16x4 k2 = (t & 1) ? __builtin_shufflevector(k2p, k2p, 0, 1, 2, 3) : __builtin_shufflevector(k2p, k2p, 4, 5, 6, 7);
        AT_GROUP("v_mfma_f32_32x32x16_bf16 %0, %7, %8, 0", "=&v"(sb), k1, q1, p1, 2, 0);
        XL(1) k1 = lds128((t < 6 ? ck1 : nk1) + TOFF((t + 2) & 7));
        AT_GROUP("v_mfma_f32_32x32x16_bf16 %0, %7, %8, %0", "+v"(o), v1, p1, p0, 0, 4);
        XL(2) v1 = lds128(cv + TOFF(t) + 2 * VB);
        XL(8) if (ld && wave >= 4) glds16(src, dst);
        AT_GROUP("v_mfma_f32_32x32x8_bf16 %0, %7, %8, %0", "+v"(sb), k2, q2, p0, 2, 8);
        XL(4) if (!(t & 1)) k2p = __builtin_bit_cast(s16x8, lds128((t < 6 ? ck2 : nk2) + TOFF((t + 2) & 7)));
        AT_GROUP("v_mfma_f32_32x32x16_bf16 %0, %7, %8, %0", "+v"(o), v0, p0, p1, 0, 12);
        XL(2) v0 = lds128((t < 7 ? cv : nv) + TOFF((t + 1) & 7));
        sa = sb;
      }
      cur = nxt; nxt = nn;
    }
    asm volatile("s_nop 7" : "+v"(o), "+v"(e[3]));
    p1[2] = pk2(e[0], e[1]); p1[3] = pk2(e[2], e[3]);
    o = MFMA32(v1, __builtin_bit_cast(bf16x8, p1), o);
    if (attempt == 1) break;
    bool ok = true;
#pragma unroll
    for (int i = 0; i < 12; i++) ok = ok && (fabsf(o[i]) < 1e30f || (hh && i > 8));
    LGKM_BAR();
    if (tid == 0) *flag = 0;
    LGKM_BAR();
    if (!ok) *flag = 1;
    LGKM_BAR();
    if (*flag == 0) break;
    if (hh) q2u = u32x2{0u, 0u};
    const s16x4 qz = __builtin_bit_cast(s16x4, q2u);
    float mx = -INFINITY;
    for (int c = 0; c < 8; c++) {
      LGKM_BAR();
      dma(c, 0);
      WAIT_BAR(0);
      for (int t = 0; t < 8; t++) { KFrag k = ldk(t); mx = fmaxf(mx, max16(qk_tile(k, q1, qz))); }
    }
    set_ref(xhalf_max(mx));
    LGKM_BAR();
    dma(0, 0); dma(1, AT_BUF);
    WAIT_BAR(0);
  }
  STAMP(1, 2);
  auto sw = __builtin_amdgcn_permlane32_swap(__float_as_uint(o[8]), __float_as_uint(o[8]), false, false);
  const float inv = 1.0f / __uint_as_float(sw[1]);
  const int s = qb * 256 + wave * 32 + r;
  char* ab = ws + WS_A3 + ((size_t)(s >> 6) * KS3 * 4 + ((s >> 4) & 3)) * 1024 + (s & 15) * 16;
#pragma unroll
  for (int i = 0; i < 3; i++) {
    if (i == 2 && hh) break;
    const int c = head * 5 + 2 * i + hh, g = 2 * ((c >> 2) & 1) + (c & 1), a = (c >> 1) & 1;
    u32x2 w = {pk2(o[4 * i] * inv, o[4 * i + 1] * inv), pk2(o[4 * i + 2] * inv, o[4 * i + 3] * inv)};
    ST8(ab + (c >> 3) * 4096 + g * 256 + a * 8, w);
  }
}

__device__ __forceinline__ void down_role(char* smem, const u16* __restrict__ x, char* __restrict__ ws, int rb, int nb, int tid, int wave PF_ARG) {
  const int lane = tid & 63;
  const char* ap = ws + WS_A3 + ((size_t)rb * KS3 + 20 + wave) * 4096 + lane * 16;
  const char* wp = ws + WS_W3 + ((size_t)nb * KS3 + 20 + wave) * 5120 + lane * 16;
  bf16x8 a[4], w[5];
#pragma unroll
  for (int t = 0; t < 5; t++) w[t] = *(const bf16x8*)(wp + t * 1024);
#pragma unroll
  for (int i = 0; i < 4; i++) a[i] = *(const bf16x8*)(ap + i * 1024);
  f32x4 acc[20] = {};
  const int n = wave < 6 ? 7 : 6;
  for (int it = 1; it <= n; it++) {
    if (it < n) { ap += 8 * 4096; wp += 8 * 5120; }
#pragma unroll
    for (int i = 0; i < 4; i++) {
#pragma unroll
      for (int t = 0; t < 5; t++) acc[t * 4 + i] = MFMA16(w[t], a[i], acc[t * 4 + i]);
      SBAR();
      a[i] = *(const bf16x8*)(ap + i * 1024);
    }
#pragma unroll
    for (int t = 0; t < 5; t++) w[t] = *(const bf16x8*)(wp + t * 1024);
  }
  STAMP(1, 2);
  f32x4* red = (f32x4*)smem;
#pragma unroll
  for (int rd = 0; rd < 3; rd++) {
    const int f = rd * 7 + wave;
    const bool on = wave < 7 && f < 20;
    const int row = rb * 64 + (f & 3) * 16 + (lane & 15);
    u32x2 xv = {0u, 0u}; float r2 = 0.f;
    if (on) { xv = *(const u32x2*)(x + (size_t)row * D_ + nb * 80 + (f >> 2) * 16 + (lane >> 4) * 4); r2 = ((const float*)(ws + WS_ST))[row]; }
    if (rd) LGKM_BAR();
#pragma unroll
    for (int u = 0; u < 7; u++)
      if (rd * 7 + u < 20) red[(wave * 7 + u) * 64 + lane] = acc[rd * 7 + u];
    LGKM_BAR();
    if (on) {
      f32x4 v = red[wave * 64 + lane];
#pragma unroll
      for (int k = 1; k < 8; k++) v += red[(k * 7 + wave) * 64 + lane];
      v = v * r2 + f32x4{bflo(xv[0]), bfhi(xv[0]), bflo(xv[1]), bfhi(xv[1])};
      ST16(ws + WS_P3 + ((size_t)(rb * 8 + nb) * 20 + f) * 1024 + lane * 16, __builtin_bit_cast(u32x4, v));
    }
  }
}

__global__ __launch_bounds__(512, 4) void k2_attn_down(const u16* __restrict__ x, char* __restrict__ ws) {
  __shared__ __attribute__((aligned(16))) char smem[K2_LDS];
  const int tid = threadIdx.x;
  const int wave = __builtin_amdgcn_readfirstlane(tid >> 6);
  const int b = blockIdx.x, xcd = b & 7, o = (b >> 3) & 31;
  PF_DECL; STAMP(1, 0);
  if (b < 256) {
    attn_role(smem, ws, xcd * 4 + (o >> 3), o & 7, tid, wave PF_PASS);
  } else {
#define X_SLEEP 64
    __builtin_amdgcn_s_sleep(X_SLEEP);
    down_role(smem, x, ws, (xcd & 3) * 8 + (o & 7), (xcd >> 2) * 4 + (o >> 3), tid, wave PF_PASS);
  }
  STAMP(1, 3);
}

__global__ __launch_bounds__(512) void k3_out(const char* __restrict__ ws, u16* __restrict__ out) {
  __shared__ __attribute__((aligned(16))) char smem[4 * 20480];
  const int tid = threadIdx.x, lane = tid & 63;
  const int wave = __builtin_amdgcn_readfirstlane(tid >> 6);
  const int xcd = blockIdx.x & 7, o = blockIdx.x >> 3, rb = (xcd & 3) * 8 + (o & 7), nb = (xcd >> 2) * 4 + (o >> 3);
  const int rh = wave & 1, kg = wave >> 1;
  PF_DECL; STAMP(2, 0);
  const char* ap = ws + WS_A3 + ((size_t)rb * KS3 + kg * 5) * 4096 + rh * 2048 + lane * 16;
  const char* wp = ws + WS_W3 + ((size_t)nb * KS3 + kg * 5) * 5120 + lane * 16;
  bf16x8 a[5][2], w[5][5];
  f32x4 acc[5][2] = {};
#pragma unroll
  for (int k = 0; k < 5; k++) {
#pragma unroll
    for (int i = 0; i < 2; i++) a[k][i] = *(const bf16x8*)(ap + k * 4096 + i * 1024);
#pragma unroll
    for (int t = 0; t < 5; t++) w[k][t] = *(const bf16x8*)(wp + k * 5120 + t * 1024);
  }
  const char* pp = ws + WS_P3 + ((size_t)(rb * 8 + nb) * 20 + rh * 2) * 1024 + lane * 16;
  f32x4 pv[2], pw[2] = {};
#pragma unroll
  for (int i = 0; i < 2; i++) { pv[i] = *(const f32x4*)(pp + kg * 4096 + i * 1024); if (kg == 0) pw[i] = *(const f32x4*)(pp + 16384 + i * 1024); }
  SBAR();
#pragma unroll
  for (int k = 0; k < 5; k++)
#pragma unroll
    for (int t = 0; t < 5; t++)
#pragma unroll
      for (int i = 0; i < 2; i++) acc[t][i] = MFMA16(w[k][t], a[k][i], acc[t][i]);
  STAMP(2, 2);
  f32x4* red = (f32x4*)smem;
#pragma unroll
  for (int t = 0; t < 5; t++)
#pragma unroll
    for (int i = 0; i < 2; i++) {
      f32x4 v = acc[t][i];
      if (t == 4) v += pw[i]; else if (t == kg) v += pv[i];
      red[kg * 1280 + (t * 4 + rh * 2 + i) * 64 + (lane & 48) + ((lane + 2 * t + (lane >> 5)) & 15)] = v;
    }
  LGKM_BAR();
#pragma unroll
  for (int u = 0; u < 2; u++) {
    const int idx = tid + u * 512;
    if (idx < 640) {
      const int row = (idx * 6554) >> 16, c = idx - row * 10;
      const f32x4* p = red + ((c >> 1) * 4 + (row >> 4)) * 64 + (c & 1) * 32 + ((row + c) & 15);
      f32x4 v0 = (p[0] + p[1280]) + (p[2560] + p[3840]), v1 = (p[16] + p[1296]) + (p[2576] + p[3856]);
      *(u32x4*)(out + (size_t)(rb * 64 + row) * D_ + nb * 80 + c * 8) = u32x4{pk2(v0[0], v0[1]), pk2(v0[2], v0[3]), pk2(v1[0], v1[1]), pk2(v1[2], v1[3])};
    }
  }
  STAMP(2, 3);
}

extern "C" void kernel_launch(void* const* d_in, const int* in_sizes, int n_in, void* d_out, int out_size, void* d_ws, size_t ws_size,
                              hipStream_t stream) {
  const u16* x = (const u16*)d_in[0];
  char* ws = (char*)d_ws;
  k1_proj<<<256, 512, 0, stream>>>(x, (const u16*)d_in[1], (const u16*)d_in[4], (const u16*)d_in[5], (const u16*)d_in[2], (const u16*)d_in[6], ws);
  k2_attn_down<<<512, 512, 0, stream>>>(x, ws);
  k3_out<<<256, 512, 0, stream>>>(ws, (u16*)d_out);
}
```

```cpp
#include <hip/hip_runtime.h>
#include <hip/hip_bf16.h>

typedef unsigned short u16;
typedef __attribute__((ext_vector_type(8))) __bf16 bf16x8;
typedef __attribute__((ext_vector_type(2))) __bf16 bf16x2;
typedef __attribute__((ext_vector_type(4))) short s16x4;
typedef __attribute__((ext_vector_type(8))) short s16x8;
typedef __attribute__((ext_vector_type(16))) float f32x16;
typedef __attribute__((ext_vector_type(4))) float f32x4;
typedef __attribute__((ext_vector_type(2))) float f32x2;
typedef __attribute__((ext_vector_type(4))) unsigned u32x4;
typedef __attribute__((ext_vector_type(2))) unsigned u32x2;
#define LAS __attribute__((address_space(3)))
typedef LAS const char* lds_cptr;
typedef __attribute__((address_space(1))) unsigned gu32;

constexpr int S_ = 2048, D_ = 640, F_ = 1728, N1_ = 1920;
constexpr int QT = 1280;
constexpr int VB = 352;
constexpr int KVT = 1024 + 4 * VB;
constexpr int KVP = 2 * KVT + 512;
constexpr int KVH = 32 * KVP;
constexpr int KS3 = 74;
constexpr size_t WS_ST = 0;
constexpr size_t WS_Q = WS_ST + 16384;
constexpr size_t WS_KV = WS_Q + (size_t)32 * 64 * QT;
constexpr size_t WS_A3 = WS_KV + (size_t)32 * KVH;
constexpr size_t WS_W3 = WS_A3 + (size_t)32 * KS3 * 4096;
constexpr size_t WS_CNT = WS_W3 + (size_t)8 * KS3 * 5120;

#define MFMA32(a, b, c) __builtin_amdgcn_mfma_f32_32x32x16_bf16(a, b, c, 0, 0, 0)
#define MFMA32X8(a, b, c) __builtin_amdgcn_mfma_f32_32x32x8bf16_1k(a, b, c, 0, 0, 0)
#define MFMA16(a, b, c) __builtin_amdgcn_mfma_f32_16x16x32_bf16(a, b, c, 0, 0, 0)
#define SGB(m, n) __builtin_amdgcn_sched_group_barrier(m, n, 0)
#define ST16(p, v) (*(u32x4*)(p) = (v))
#define ST8(p, v) (*(u32x2*)(p) = (v))
#define SBAR() __builtin_amdgcn_sched_barrier(0)
#define PIN(x) asm volatile("" : "+v"(x))
#define WAIT_BAR(N) asm volatile("s_waitcnt vmcnt(" #N ") lgkmcnt(0)\n\ts_barrier" ::: "memory")
#define LGKM_BAR() asm volatile("s_waitcnt lgkmcnt(0)\n\ts_barrier" ::: "memory")

#define X_PH 2
#define X_POLL 4
#define X_LM 0
#define XL(b) if (!(X_LM & b))
#define PF_DECL
#define PF_ARG
#define PF_PASS
#define STAMP(k, i)
__device__ __forceinline__ void glds16(const void* g, unsigned lds_base) {
  asm volatile("s_mov_b32 m0, %1\n\ts_nop 0\n\tglobal_load_lds_dwordx4 %0, off" :: "v"(g), "s"(lds_base) : "memory", "m0");
}
__device__ __forceinline__ unsigned pk2(float lo, float hi) {
  f32x2 v = {lo, hi};
  return __builtin_bit_cast(unsigned, __builtin_convertvector(v, bf16x2));
}
__device__ __forceinline__ float bfr(float v) { return (float)(__bf16)v; }
__device__ __forceinline__ float bflo(unsigned u) { return __uint_as_float(u << 16); }
__device__ __forceinline__ float bfhi(unsigned u) { return __uint_as_float(u & 0xffff0000u); }
__device__ __forceinline__ bf16x8 lds128(lds_cptr p) { return *(const LAS bf16x8*)p; }
__device__ __forceinline__ s16x4 lds64(lds_cptr p) { return *(const LAS s16x4*)p; }
__device__ __forceinline__ bf16x8 ldstr2(lds_cptr p, int second) {
  s16x4 a = __builtin_amdgcn_ds_read_tr16_b64_v4i16((LAS s16x4*)p);
  s16x4 b = __builtin_amdgcn_ds_read_tr16_b64_v4i16((LAS s16x4*)(p + second));
  return __builtin_bit_cast(bf16x8, __builtin_shufflevector(a, b, 0, 1, 2, 3, 4, 5, 6, 7));
}
__device__ __forceinline__ float xhalf_max(float v) {
  auto r = __builtin_amdgcn_permlane32_swap(__float_as_uint(v), __float_as_uint(v), false, false);
  return fmaxf(__uint_as_float(r[0]), __uint_as_float(r[1]));
}
__device__ __forceinline__ void ln_stats(float sum, float ssq, float& mean_b, float& std_b) {
  float mean = sum * (1.0f / D_);
  float var = fmaxf(ssq * (1.0f / D_) - mean * mean, 0.0f);
  mean_b = bfr(mean);
  std_b = bfr(sqrtf(bfr(bfr(var) + 1e-5f)));
}

constexpr int XSLOT = 32768, WSLOT = 12288, K1_X0 = 4 * WSLOT, K1_LDS = 3 * XSLOT + 4 * WSLOT;

template <int MODE>
struct K1Core {
  static constexpr int NB = MODE == 2 ? 6 : 5;
  f32x4 acc[NB][4], cs[2], rs[2], sq[2];
  bf16x8 xf[2][4], wf[2][NB], sel;
  const u16 *xs0, *xs1, *wpa, *wpb;
  int wstep, xo, wo[5], wave;
  unsigned lds0;
  lds_cptr lb;
  template <int PAR> __device__ __forceinline__ void issue_x(int h, int xslot) {
    const int off = ((h + 5) >> 1) * 64 + (PAR ^ 1) * 128 * D_;
    const unsigned dst = lds0 + K1_X0 + xslot + (PAR ^ 1) * 16384 + wave * 2048;
    glds16(xs0 + off, dst); glds16(xs1 + off, dst + 1024);
  }
  template <int PAR, int WM> __device__ __forceinline__ void issue_w(int h) {
    glds16(wpa + (h + 4) * wstep, lds0 + (h & 3) * WSLOT + wave * 1024);
    if ((WM >> 1) == PAR) glds16(wpb + (h + 4) * wstep, lds0 + (h & 3) * WSLOT + (8 + (wave & 3)) * 1024);
  }
  template <int PAR> __device__ __forceinline__ void load(int wslot, int xslot) {
    lds_cptr xb = lb + K1_X0 + xslot + (xo ^ (PAR * 64)), xc = lb + K1_X0 + xslot + (xo ^ (PAR * 64) ^ 4096);
#pragma unroll
    for (int j = 0; j < NB; j++) {
      if (j < 4) xf[PAR][j] = lds128((j < 2 ? xb : xc) + (j & 1) * 2048);
      wf[PAR][j] = ldstr2(lb + wslot + (MODE == 2 ? wo[j % 3] + (j / 3) * 6144 : wo[j]), 256);
    }
  }
  template <int PAR, int WM> __device__ __forceinline__ void mma(int j0, int j1) {
#pragma unroll
    for (int j = j0; j < j1; j++) {
#pragma unroll
      for (int a = 0; a < 4; a++) acc[j][a] = MODE == 1 ? MFMA16(xf[PAR][a], wf[PAR][j], acc[j][a]) : MFMA16(wf[PAR][j], xf[PAR][a], acc[j][a]);
      if ((j & 3) == WM) cs[j >> 2] = MFMA16(sel, wf[PAR][j], cs[j >> 2]);
    }
  }
  template <int PAR, int WM> __device__ __forceinline__ void step(int h, int xslot_w, int xslot_next, int wslot_next) {
    if (h < 16) WAIT_BAR(7); else WAIT_BAR(0);
    if (WM < 2) { if (h < 15) issue_x<PAR>(h, xslot_w); if (X_PH == 1 && h < 16) issue_w<PAR, WM>(h); }
    asm volatile("" : "+v"(xf[PAR][0]), "+v"(xf[PAR][1]), "+v"(xf[PAR][2]), "+v"(xf[PAR][3]));
    SBAR();
    load<PAR ^ 1>(wslot_next, xslot_next);
    mma<PAR, WM>(0, 4);
    SGB(0x2, 2);
#pragma unroll
    for (int i = 0; i < 4 + 2 * NB; i++) { SGB(0x8, 1); SGB(0x2, 1); SGB(0x100, 1); }
    SBAR();
#pragma unroll
    for (int j = 0; j < 4; j++) asm volatile("" : "+v"(acc[j][0]), "+v"(acc[j][1]), "+v"(acc[j][2]), "+v"(acc[j][3]));
    asm volatile("" : "+v"(cs[0]), "+v"(xf[PAR][0]), "+v"(xf[PAR][1]), "+v"(xf[PAR][2]), "+v"(xf[PAR][3]));
    if (X_PH == 2 && h < 16) issue_w<PAR, WM>(h);
    SBAR();
    mma<PAR, WM>(4, NB);
#pragma unroll
    for (int a = 0; a < 2; a++) {
      rs[a] = MFMA16(sel, xf[PAR][a], rs[a]);
      if (MODE != 2) sq[a] = MFMA16(xf[PAR][a], xf[PAR][a], sq[a]);
    }
    SBAR();
#pragma unroll
    for (int j = 4; j < NB; j++) asm volatile("" : "+v"(acc[j][0]), "+v"(acc[j][1]), "+v"(acc[j][2]), "+v"(acc[j][3]));
    asm volatile("" : "+v"(cs[1]), "+v"(rs[0]), "+v"(rs[1]));
    if (MODE != 2) asm volatile("" : "+v"(sq[0]), "+v"(sq[1]));
    if (WM >= 2) { if (h < 15) issue_x<PAR>(h, xslot_w); if (X_PH == 1 && h < 16) issue_w<PAR, WM>(h); }
  }
  template <int WM> __device__ __forceinline__ void run(char* ws PF_ARG) {
    issue_x<1>(-5, 0); issue_x<0>(-4, 0); issue_w<0, WM>(-4);
    issue_x<1>(-3, XSLOT); issue_w<1, WM>(-3); issue_x<0>(-2, XSLOT); issue_w<0, WM>(-2);
#pragma unroll
    for (int j = 0; j < NB; j++) for (int a = 0; a < 4; a++) acc[j][a] = f32x4{};
    cs[0] = cs[1] = rs[0] = rs[1] = sq[0] = sq[1] = f32x4{};
    STAMP(0, 4);
    WAIT_BAR(7);
    STAMP(0, 5);
    load<0>(0, 0);
    issue_x<1>(-1, 2 * XSLOT); issue_w<1, WM>(-1);
    int s0 = 0, s1 = XSLOT, s2 = 2 * XSLOT;
    for (int h = 0; h < 20; h += 2) {
      const int wb = (h & 2) * WSLOT;
      step<0, WM>(h, s2, s0, wb + WSLOT);
      step<1, WM>(h + 1, s0, s1, wb ^ (2 * WSLOT));
      const int t = s0; s0 = s1; s1 = s2; s2 = t;
    }
  }
};

template <int MODE>
__device__ __forceinline__ void k1_tile(char* smem, const u16* __restrict__ x, const u16* __restrict__ w0, const u16* __restrict__ w1,
                                        char* __restrict__ ws, int rb, int ct, int tid, int wave PF_ARG) {
  constexpr int NB = K1Core<MODE>::NB, LD = MODE == 2 ? F_ : N1_;
  const int lane = tid & 63, i = lane & 15, g = lane >> 4, wm = wave >> 1, wn = wave & 1;
  K1Core<MODE> c;
  c.wave = wave; c.lds0 = (unsigned)(uintptr_t)smem; c.lb = (lds_cptr)smem; c.wstep = 32 * LD;
  c.xs0 = x + (size_t)(rb * 256 + wave * 16 + (lane >> 3)) * D_ + (((lane & 7) ^ g) << 3);
  c.xs1 = x + (size_t)(rb * 256 + wave * 16 + 8 + (lane >> 3)) * D_ + (((lane & 7) ^ (4 + g)) << 3);
  {
    const int kr = (wave & 1) * 16 + (lane >> 2), ch = ((lane & 3) ^ ((lane >> 5) << 1)) << 3;
    const int ua = wave >> 1, ub = 4 + ((wave & 3) >> 1);
    if (MODE == 2) {
      c.wpa = (ua < 3 ? w0 + ua * 32 : w1) + (size_t)kr * LD + ct * 96 + ch;
      c.wpb = w1 + (ub - 3) * 32 + (size_t)kr * LD + ct * 96 + ch;
    } else {
      c.wpa = w0 + ua * 32 + (size_t)kr * LD + ct * 160 + ch;
      c.wpb = w0 + 128 + (size_t)kr * LD + ct * 160 + ch;
    }
  }
  c.xo = (wm * 64 + wn * 32 + i) * 128 + ((g ^ (i >> 1)) << 4);
  {
    const int q = i >> 2, p = lane & 3;
#pragma unroll
    for (int j = 0; j < 5; j++) {
      const int Q = MODE == 0 ? 5 * p + j + 20 * wn : MODE == 1 ? 4 * j + p + 20 * wn : 4 * (wn ? (j == 2 ? 3 : 4 + j) : j) + p;
      c.wo[j] = (Q >> 3) * 2048 + (g >> 1) * 1024 + (8 * (g & 1) + q) * 64 + (((Q & 7) ^ (4 * (g & 1))) << 3);
    }
    const unsigned v = i == 0 ? 0x3f803f80u : 0u;
    c.sel = __builtin_bit_cast(bf16x8, u32x4{v, v, v, v});
  }
  if (wm == 0) c.template run<0>(ws PF_PASS);
  else if (wm == 1) c.template run<1>(ws PF_PASS);
  else if (wm == 2) c.template run<2>(ws PF_PASS);
  else c.template run<3>(ws PF_PASS);
  STAMP(0, 2);
  float* sc = (float*)smem;
  if (g == 0) {
#pragma unroll
    for (int a = 0; a < 2; a++) sc[wm * 64 + wn * 32 + a * 16 + i] = c.rs[a][0];
    sc[1792 + wn * 96 + wm * 16 + i] = c.cs[0][0];
    if (wm + 4 < NB) sc[1792 + wn * 96 + (wm + 4) * 16 + i] = c.cs[1][0];
  }
  if (MODE != 2 && g == (i >> 2)) {
#pragma unroll
    for (int a = 0; a < 2; a++) {
      const float s01 = (i & 1) ? c.sq[a][1] : c.sq[a][0], s23 = (i & 1) ? c.sq[a][3] : c.sq[a][2];
      sc[256 + wm * 64 + wn * 32 + a * 16 + i] = (i & 2) ? s23 : s01;
    }
  }
  LGKM_BAR();
  if (tid < 256) {
    if (MODE == 2) {
      sc[1536 + tid] = -bfr(sc[tid] * (1.0f / D_));
    } else {
      float mean_b, std_b;
      ln_stats(sc[tid], sc[256 + tid], mean_b, std_b);
      const float r = 1.0f / std_b;
      sc[1280 + tid] = r; sc[1536 + tid] = -mean_b * r;
      if (ct == 0) ((float*)(ws + WS_ST))[rb * 256 + tid] = r * r;
    }
  }
  LGKM_BAR();
  if (MODE == 0) {
    const bool isq = ct < 4;
    const float scl = isq ? 0.22360679774997896f * 1.4426950408889634f : 1.0f;
    const int head = (ct & 3) * 8 + wn * 4 + g;
    f32x4 cv[5];
#pragma unroll
    for (int j = 0; j < 5; j++) cv[j] = *(const f32x4*)(sc + 1792 + wn * 96 + j * 16 + 4 * g);
#pragma unroll
    for (int a = 0; a < 4; a++) {
      const int row = wm * 64 + ((a * 16) ^ (wn * 32)) + i, odd = (a >> 1) ^ wn;
      const float r = sc[1280 + row] * scl, m = sc[1536 + row] * scl;
      unsigned w[10];
#pragma unroll
      for (int j = 0; j < 5; j++) {
        w[2 * j] = pk2(c.acc[j][a][0] * r + m * cv[j][0], c.acc[j][a][1] * r + m * cv[j][1]);
        w[2 * j + 1] = pk2(c.acc[j][a][2] * r + m * cv[j][2], c.acc[j][a][3] * r + m * cv[j][3]);
      }
      const int rr = isq ? (a & 1) * 16 + i : 16 * (i >> 3) + 8 * (a & 1) + (i & 7);
      char* tb = isq ? ws + WS_Q + (size_t)(head * 64 + rb * 8 + wm * 2 + odd) * QT : ws + WS_KV + (size_t)head * KVH + (rb * 4 + wm) * KVP + odd * KVT;
      ST16(tb + rr * 16, (u32x4{w[0], w[1], w[2], w[3]}));
      ST16(tb + 512 + rr * 16, (u32x4{w[4], w[5], w[6], w[7]}));
      ST8(tb + (isq ? 1024 + rr * 8 : 2 * KVT - odd * (KVT - 8) + rr * 16), (u32x2{w[8], w[9]}));
    }
  } else if (MODE == 1) {
#pragma unroll
    for (int ap = 0; ap < 2; ap++) {
      const int odd = ap ^ wn, row = wm * 64 + odd * 32 + 4 * g;
      const f32x4 r0 = *(const f32x4*)(sc + 1280 + row), r1 = *(const f32x4*)(sc + 1296 + row);
      const f32x4 m0 = *(const f32x4*)(sc + 1536 + row), m1 = *(const f32x4*)(sc + 1552 + row);
#pragma unroll
      for (int j = 0; j < 5; j++) {
        const int n = 16 * j + i, hl = (n * 52429) >> 20, d = n - hl * 20;
        const float cv = sc[1792 + wn * 96 + n];
        const f32x4 u = c.acc[j][2 * ap] * r0 + m0 * cv, v = c.acc[j][2 * ap + 1] * r1 + m1 * cv;
        char* tb = ws + WS_KV + (size_t)((ct & 3) * 8 + wn * 4 + hl) * KVH + (rb * 4 + wm) * KVP + odd * KVT + 1024 + g * VB + d * 16;
        ST16(tb, (u32x4{pk2(u[0], u[1]), pk2(u[2], u[3]), pk2(v[0], v[1]), pk2(v[2], v[3])}));
        if (j > 0 && d >= 18) { const unsigned k = (d == 18) ? 0x3f803f80u : 0u; ST16(tb + 32, (u32x4{k, k, k, k})); }
      }
    }
  } else {
    f32x4 cv[6];
#pragma unroll
    for (int j = 0; j < 6; j++) cv[j] = *(const f32x4*)(sc + 1792 + wn * 96 + j * 16 + 4 * g);
#pragma unroll
    for (int a = 0; a < 4; a++) {
      const int sub = a ^ (2 * wn);
      const float m = sc[1536 + wm * 64 + sub * 16 + i];
      unsigned w[6];
#pragma unroll
      for (int j = 0; j < 3; j++)
#pragma unroll
        for (int e = 0; e < 2; e++) {
          const unsigned gp = pk2(c.acc[j][a][2 * e] + m * cv[j][2 * e], c.acc[j][a][2 * e + 1] + m * cv[j][2 * e + 1]);
          const unsigned up = pk2(c.acc[j + 3][a][2 * e] + m * cv[j + 3][2 * e], c.acc[j + 3][a][2 * e + 1] + m * cv[j + 3][2 * e + 1]);
          w[2 * j + e] = pk2(bflo(gp) * bflo(up), bfhi(gp) * bfhi(up));
        }
      char* ab = ws + WS_A3 + (((size_t)(rb * 4 + wm) * KS3 + 20 + ct * 3) * 4 + sub) * 1024 + lane * 16;
      ST16(ab + wn * 8192, (u32x4{w[0], w[1], w[2], w[3]}));
      ST8(ab + 4096 + wn * 8, (u32x2{w[4], w[5]}));
    }
  }
}

__device__ __forceinline__ void retile_role(char* smem, const u16* __restrict__ wo, const u16* __restrict__ wd, char* __restrict__ ws,
                                            int idx, int tid, int wave) {
  const int lane = tid & 63, g = lane >> 4, q = (lane & 15) >> 2, p = lane & 3;
  const unsigned lds0 = (unsigned)(uintptr_t)smem;
  for (int k0 = idx; k0 < KS3; k0 += 48) {
    LGKM_BAR();
    for (int u = 0; u < 3; u++) {
      const int ks = k0 + 16 * u;
      if (ks < KS3) {
        const int orow = ks < 16 ? (2 * ks + (wave >> 2)) * 20 + (wave & 2) * 2 + (wave & 1) * 8 : (8 * (ks - 16) + wave) * 20 + 16;
        const char* src = (const char*)(ks < 20 ? wo + (size_t)orow * D_ : wd + (size_t)((ks - 20) * 32 + wave * 4) * D_) + lane * 16;
#pragma unroll
        for (int e = 0; e < 5; e++) glds16(src + e * 1024, lds0 + u * 40960 + wave * 5120 + e * 1024);
      }
    }
    WAIT_BAR(0);
    for (int u = 0; u < 3; u++) {
      const int ks = k0 + 16 * u;
      if (ks < KS3) {
        const int row = ks < 20 ? 8 * g + q : 4 * g + q, sec = (ks < 20 ? 4 : 16) * 1280;
        lds_cptr b = (lds_cptr)smem + u * 40960 + row * 1280 + wave * 160;
#pragma unroll
        for (int t = 0; t < 5; t++)
          ST16(ws + WS_W3 + (((size_t)wave * KS3 + ks) * 5 + t) * 1024 + lane * 16, __builtin_bit_cast(u32x4, ldstr2(b + (t < 4 ? (t >> 1) * 64 + p * 16 + (t & 1) * 8 : 128 + p * 8), sec)));
      }
    }
  }
}

__global__ __launch_bounds__(512) void k1_proj(const u16* __restrict__ x, const u16* __restrict__ wqkv, const u16* __restrict__ wg,
                                               const u16* __restrict__ wu, const u16* __restrict__ wo, const u16* __restrict__ wd,
                                               char* __restrict__ ws) {
  __shared__ __attribute__((aligned(16))) char smem[K1_LDS];
  const int tid = threadIdx.x;
  const int wave = __builtin_amdgcn_readfirstlane(tid >> 6);
  const int xcd = blockIdx.x & 7, o = blockIdx.x >> 3, cg = xcd >> 1, t = o >> 2;
  const int rb = (xcd & 1) * 4 + (o & 3);
  PF_DECL; STAMP(0, 0);
  if (t < 3) {
    const int ct = cg * 3 + t;
    if (ct < 8) k1_tile<0>(smem, x, wqkv, wqkv, ws, rb, ct, tid, wave PF_PASS);
    else k1_tile<1>(smem, x, wqkv, wqkv, ws, rb, ct, tid, wave PF_PASS);
  } else if (t < 7 || !(cg & 1)) {
    k1_tile<2>(smem, x, wg, wu, ws, rb, (cg >> 1) * 9 + (cg & 1) * 5 + t - 3, tid, wave PF_PASS);
  } else {
    if (cg == 1 && rb == 0 && tid < 32) ((unsigned*)(ws + WS_CNT))[tid * 64] = 0u;
    retile_role(smem, wo, wd, ws, (cg >> 1) * 8 + rb, tid, wave);
  }
  STAMP(0, 3);
}

constexpr int AT_BUF = 4 * KVP;
constexpr int K2_LDS = 81920;

#define TOFF(t) (((t) >> 1) * KVP + ((t) & 1) * KVT)
struct KFrag { bf16x8 k1; s16x4 k2; };

__device__ __forceinline__ f32x16 qk_tile(const KFrag& k, bf16x8 q1, s16x4 q2) {
  f32x16 z = {};
  f32x16 s = MFMA32(k.k1, q1, z);
  return MFMA32X8(k.k2, q2, s);
}
__device__ __forceinline__ bf16x8 exp_pack(const f32x16& s, int m) {
  u32x4 w;
#pragma unroll
  for (int i = 0; i < 4; i++) w[i] = pk2(__builtin_amdgcn_exp2f(s[8 * m + 2 * i]), __builtin_amdgcn_exp2f(s[8 * m + 2 * i + 1]));
  return __builtin_bit_cast(bf16x8, w);
}
__device__ __forceinline__ float max16(const f32x16& s) {
  float a = fmaxf(s[0], s[1]);
#pragma unroll
  for (int i = 2; i < 16; i += 2) a = fmaxf(fmaxf(a, s[i]), s[i + 1]);
  return a;
}

#define AT_GROUP(MF, ACC, A, B, P, PJ, SJ) do { unsigned pa_, pb_; SBAR();                                                        \
    asm volatile(MF "\n\tv_cvt_pk_bf16_f32 %1, %3, %4\n\tv_cvt_pk_bf16_f32 %2, %5, %6\n\tv_exp_f32 %3, %9\n\tv_exp_f32 %4, %10\n\t"    \
                 "v_exp_f32 %5, %11\n\tv_exp_f32 %6, %12"                                                                          \
                 : ACC, "=&v"(pa_), "=&v"(pb_), "+v"(e[0]), "+v"(e[1]), "+v"(e[2]), "+v"(e[3])                                     \
                 : "v"(A), "v"(B), "v"(sa[SJ]), "v"(sa[SJ + 1]), "v"(sa[SJ + 2]), "v"(sa[SJ + 3]));                                \
    P[PJ] = pa_; P[PJ + 1] = pb_; SBAR(); } while (0)

__device__ __forceinline__ void attn_role(char* smem, char* __restrict__ ws, int head, int qb, int tid, int wave PF_ARG) {
  const int lane = tid & 63, r = lane & 31, hh = lane >> 5;
  const unsigned lds0 = (unsigned)(uintptr_t)smem;
  const char* kvsrc = ws + WS_KV + (size_t)head * KVH + wave * 1024 + lane * 16;
  auto dma = [&](int c, int buf) {
    const char* src = kvsrc + (size_t)c * AT_BUF;
    const unsigned dst = lds0 + buf + wave * 1024;
    glds16(src, dst);
    glds16(src + 8192, dst + 8192);
    if (wave < 5) glds16(src + 16384, dst + 16384);
  };
  dma(0, 0);
  const char* qp = ws + WS_Q + ((size_t)head * 64 + qb * 8 + wave) * QT;
  const bf16x8 q1 = *(const bf16x8*)(qp + lane * 16);
  u32x2 q2u = *(const u32x2*)(qp + 1024 + r * 8);
  if (hh) q2u = u32x2{0u, 0u};
  const int k1o = lane * 16, k2o = hh ? 1024 + 320 : 2 * KVT + r * 16, vo = 1024 + hh * VB + min(r, 21) * 16;
  lds_cptr lb = (lds_cptr)smem;
  LAS int* flag = (LAS int*)(lb + 3 * AT_BUF + 512);
  auto ldk = [&](int t) { KFrag k; k.k1 = lds128(lb + TOFF(t) + k1o); k.k2 = lds64(lb + (t >> 1) * KVP + (t & 1) * 8 + k2o); return k; };
  auto set_ref = [&](float m) {
    const float m0 = bfr(m), m1 = bfr(m - m0), m2 = bfr(m - m0 - m1);
    if (hh) q2u = u32x2{pk2(-m0, -m1), pk2(-m2, 0.f)};
  };
  WAIT_BAR(0);
  dma(1, AT_BUF);
  {
    KFrag k = ldk(0);
    set_ref(xhalf_max(max16(qk_tile(k, q1, __builtin_bit_cast(s16x4, q2u)))));
  }
  f32x16 o;
  STAMP(1, 1);
  for (int attempt = 0;; attempt++) {
    const s16x4 q2 = __builtin_bit_cast(s16x4, q2u);
    o = f32x16{};
    f32x16 sa;
    { KFrag k = ldk(0); sa = qk_tile(k, q1, q2); }
    bf16x8 k1 = lds128(lb + KVT + k1o), v1 = {}, v0;
    s16x8 k2p = __builtin_bit_cast(s16x8, lds128(lb + k2o));
    v0 = lds128(lb + vo);
    u32x4 p0 = {0u, 0u, 0u, 0u}, p1 = {0u, 0u, 0u, 0u};
    float e[4] = {0.f, 0.f, 0.f, 0.f};
    int cur = 0, nxt = AT_BUF;
    asm volatile("s_nop 15\n\ts_nop 7" : "+v"(sa), "+v"(o), "+v"(p1), "+v"(v1));
    for (int c = 0; c < 8; c++) {
      lds_cptr ck1 = lb + cur + k1o, ck2 = lb + cur + k2o, cv = lb + cur + vo;
      lds_cptr nk1 = lb + nxt + k1o, nk2 = lb + nxt + k2o, nv = lb + nxt + vo;
      const int nn = (nxt == 2 * AT_BUF) ? 0 : nxt + AT_BUF;
#pragma unroll
      for (int t = 0; t < 8; t++) {
        XL(8) if (t == 6) asm volatile("s_waitcnt vmcnt(0)\n\ts_barrier" ::: "memory");
        const int pc = (t + 2) & 7;
        const bool ld = pc < 3 && (pc < 2 ? c < 6 : (c > 0 && c < 7 && wave < 5));
        const char* src = kvsrc + (size_t)(t == 0 ? c + 1 : c + 2) * AT_BUF + pc * 8192;
        const unsigned dst = lds0 + (t == 0 ? nxt : nn) + wave * 1024 + pc * 8192;
        XL(8) if (ld && wave < 4) glds16(src, dst);
        f32x16 sb;
        const s16x4 k2 = (t & 1) ? __builtin_shufflevector(k2p, k2p, 0, 1, 2, 3) : __builtin_shufflevector(k2p, k2p, 4, 5, 6, 7);
        AT_GROUP("v_mfma_f32_32x32x16_bf16 %0, %7, %8, 0", "=&v"(sb), k1, q1, p1, 2, 0);
        XL(1) k1 = lds128((t < 6 ? ck1 : nk1) + TOFF((t + 2) & 7));
        AT_GROUP("v_mfma_f32_32x32x16_bf16 %0, %7, %8, %0", "+v"(o), v1, p1, p0, 0, 4);
        XL(2) v1 = lds128(cv + TOFF(t) + 2 * VB);
        XL(8) if (ld && wave >= 4) glds16(src, dst);
        AT_GROUP("v_mfma_f32_32x32x8_bf16 %0, %7, %8, %0", "+v"(sb), k2, q2, p0, 2, 8);
        XL(4) if (!(t & 1)) k2p = __builtin_bit_cast(s16x8, lds128((t < 6 ? ck2 : nk2) + TOFF((t + 2) & 7)));
        AT_GROUP("v_mfma_f32_32x32x16_bf16 %0, %7, %8, %0", "+v"(o), v0, p0, p1, 0, 12);
        XL(2) v0 = lds128((t < 7 ? cv : nv) + TOFF((t + 1) & 7));
        sa = sb;
      }
      cur = nxt; nxt = nn;
    }
    asm volatile("s_nop 7" : "+v"(o), "+v"(e[3]));
    p1[2] = pk2(e[0], e[1]); p1[3] = pk2(e[2], e[3]);
    o = MFMA32(v1, __builtin_bit_cast(bf16x8, p1), o);
    if (attempt == 1) break;
    bool ok = true;
#pragma unroll
    for (int i = 0; i < 12; i++) ok = ok && (fabsf(o[i]) < 1e30f || (hh && i > 8));
    LGKM_BAR();
    if (tid == 0) *flag = 0;
    LGKM_BAR();
    if (!ok) *flag = 1;
    LGKM_BAR();
    if (*flag == 0) break;
    if (hh) q2u = u32x2{0u, 0u};
    const s16x4 qz = __builtin_bit_cast(s16x4, q2u);
    float mx = -INFINITY;
    for (int c = 0; c < 8; c++) {
      LGKM_BAR();
      dma(c, 0);
      WAIT_BAR(0);
      for (int t = 0; t < 8; t++) { KFrag k = ldk(t); mx = fmaxf(mx, max16(qk_tile(k, q1, qz))); }
    }
    set_ref(xhalf_max(mx));
    LGKM_BAR();
    dma(0, 0); dma(1, AT_BUF);
    WAIT_BAR(0);
  }
  STAMP(1, 2);
  auto sw = __builtin_amdgcn_permlane32_swap(__float_as_uint(o[8]), __float_as_uint(o[8]), false, false);
  const float inv = 1.0f / __uint_as_float(sw[1]);
  const int s = qb * 256 + wave * 32 + r;
  const auto rs = __builtin_amdgcn_make_buffer_rsrc(ws + WS_A3, 0, 32 * KS3 * 4096, 0x00020000);
  const int ab = ((s >> 6) * KS3 * 4 + ((s >> 4) & 3)) * 1024;
  __builtin_amdgcn_raw_buffer_store_b128(u32x4{pk2(o[0] * inv, o[1] * inv), pk2(o[2] * inv, o[3] * inv), pk2(o[4] * inv, o[5] * inv), pk2(o[6] * inv, o[7] * inv)},
                                         rs, ab + (head >> 1) * 4096 + ((head & 1) * 2 + hh) * 256 + (s & 15) * 16, 0, 16);
  if (!hh) __builtin_amdgcn_raw_buffer_store_b64(u32x2{pk2(o[8] * inv, o[9] * inv), pk2(o[10] * inv, o[11] * inv)},
                                                 rs, ab + (16 + (head >> 3)) * 4096 + (head & 7) * 128 + (s & 15) * 8, 0, 16);
  WAIT_BAR(0);
  if (tid < 4) __hip_atomic_fetch_add((gu32*)(ws + WS_CNT) + (qb * 4 + tid) * 64, 1u, __ATOMIC_RELAXED, __HIP_MEMORY_SCOPE_AGENT);
}

__device__ __forceinline__ void down_role(char* smem, const u16* __restrict__ x, char* __restrict__ ws, u16* __restrict__ out, int rb, int nb, int tid, int wave PF_ARG) {
  const int lane = tid & 63;
  const char* ap = ws + WS_A3 + ((size_t)rb * KS3 + 20 + wave) * 4096 + lane * 16;
  const char* wp = ws + WS_W3 + ((size_t)nb * KS3 + 20 + wave) * 5120 + lane * 16;
  bf16x8 a[4], w[5];
#pragma unroll
  for (int t = 0; t < 5; t++) w[t] = *(const bf16x8*)(wp + t * 1024);
#pragma unroll
  for (int i = 0; i < 4; i++) a[i] = *(const bf16x8*)(ap + i * 1024);
  f32x4 acc[20] = {};
  const int n = wave < 6 ? 7 : 6;
  for (int it = 1; it <= n; it++) {
    if (it < n) { ap += 8 * 4096; wp += 8 * 5120; }
#pragma unroll
    for (int i = 0; i < 4; i++) {
#pragma unroll
      for (int t = 0; t < 5; t++) acc[t * 4 + i] = MFMA16(w[t], a[i], acc[t * 4 + i]);
      SBAR();
      a[i] = *(const bf16x8*)(ap + i * 1024);
    }
#pragma unroll
    for (int t = 0; t < 5; t++) w[t] = *(const bf16x8*)(wp + t * 1024);
  }
  STAMP(1, 2);
  f32x4* red = (f32x4*)smem;
  const int rbk = wave & 3, kg = wave >> 2, g = lane >> 4, row = rb * 64 + rbk * 16 + (lane & 15);
  const size_t xo = (size_t)row * D_ + nb * 80;
  const u32x4 xv = *(const u32x4*)(x + xo + kg * 32 + g * 8);
  const u32x2 xw = *(const u32x2*)(x + xo + 64 + g * 4);
  const float r2 = ((const float*)(ws + WS_ST))[row];
  f32x4 fin[3];
#pragma unroll
  for (int rd = 0; rd < 3; rd++) {
    if (rd) LGKM_BAR();
#pragma unroll
    for (int u = 0; u < (rd < 2 ? 8 : 4); u++) red[(wave * 8 + u) * 64 + lane] = acc[(rd < 2 ? 2 * (u >> 2) + rd : 4) * 4 + (u & 3)];
    LGKM_BAR();
    f32x4 v = red[(rd < 2 ? wave : rbk) * 64 + lane];
#pragma unroll
    for (int k = 1; k < 8; k++) v += red[(k * 8 + (rd < 2 ? wave : rbk)) * 64 + lane];
    const unsigned x0 = rd < 2 ? xv[2 * rd] : xw[0], x1 = rd < 2 ? xv[2 * rd + 1] : xw[1];
    fin[rd] = v * r2 + f32x4{bflo(x0), bfhi(x0), bflo(x1), bfhi(x1)};
    PIN(fin[rd]);
  }
  LGKM_BAR();
  STAMP(1, 4);
  const char* wo = ws + WS_W3 + ((size_t)nb * KS3 + kg * 10) * 5120 + lane * 16;
  const unsigned lds0 = (unsigned)(uintptr_t)smem;
#pragma unroll
  for (int e = 0; e < 10; e++) glds16(wo + (10 + rbk * 10 + e) * 1024, lds0 + (wave * 10 + e) * 1024);
  bf16x8 wr[10];
#pragma unroll
  for (int e = 0; e < 10; e++) wr[e] = *(const bf16x8*)(wo + e * 1024);
  f32x4 c[5];
  c[0] = kg ? f32x4{} : fin[0]; c[1] = kg ? f32x4{} : fin[1]; c[2] = kg ? fin[0] : f32x4{}; c[3] = kg ? fin[1] : f32x4{}; c[4] = kg ? f32x4{} : fin[2];
#pragma unroll
  for (int e = 0; e < 10; e++) PIN(wr[e]);
  asm volatile("" : "+v"(c[0]), "+v"(c[1]), "+v"(c[2]), "+v"(c[3]), "+v"(c[4]));
  STAMP(1, 5);
  if (tid == 0) {
    gu32* cnt = (gu32*)(ws + WS_CNT) + rb * 64;
    for (int i = 0; i < (1 << 22) && __hip_atomic_load(cnt, __ATOMIC_RELAXED, __HIP_MEMORY_SCOPE_AGENT) != 32u; i++) __builtin_amdgcn_s_sleep(X_POLL);
  }
  WAIT_BAR(0);
  STAMP(1, 6);
  const auto rs = __builtin_amdgcn_make_buffer_rsrc(ws + WS_A3, 0, 32 * KS3 * 4096, 0x00020000);
  const int ob = ((rb * KS3 + kg * 10) * 4 + rbk) * 1024, o2 = kg ? g * 256 + (lane & 15) * 8 : lane * 16, o3 = o2 + (kg ? 128 : 8);
  u32x4 of[10];
#pragma unroll
  for (int k = 0; k < 10; k++) {
    if (k < 6) of[k] = __builtin_amdgcn_raw_buffer_load_b128(rs, lane * 16, ob + k * 4096, 16);
    else {
      const u32x2 lo = __builtin_amdgcn_raw_buffer_load_b64(rs, o2, ob + k * 4096, 16), hi = __builtin_amdgcn_raw_buffer_load_b64(rs, o3, ob + k * 4096, 16);
      of[k] = u32x4{lo[0], lo[1], hi[0], hi[1]};
    }
  }
  SBAR();
  lds_cptr lw = (lds_cptr)smem + kg * 40960 + lane * 16;
#pragma unroll
  for (int k = 0; k < 10; k++) {
#pragma unroll
    for (int t = 0; t < 5; t++) c[t] = MFMA16(wr[(k & 1) * 5 + t], __builtin_bit_cast(bf16x8, of[k]), c[t]);
    asm volatile("" : "+v"(c[0]), "+v"(c[1]), "+v"(c[2]), "+v"(c[3]), "+v"(c[4]) :: "memory");
    if (k < 8)
#pragma unroll
      for (int t = 0; t < 5; t++) wr[(k & 1) * 5 + t] = lds128(lw + (k * 5 + t) * 1024);
    SBAR();
  }
  LGKM_BAR();
  red[(wave * 3) * 64 + lane] = kg ? c[0] : c[2];
  red[(wave * 3 + 1) * 64 + lane] = kg ? c[1] : c[3];
  if (kg) red[(wave * 3 + 2) * 64 + lane] = c[4];
  LGKM_BAR();
  const f32x4* pr = red + ((wave ^ 4) * 3) * 64 + lane;
  const f32x4 v0 = (kg ? c[2] : c[0]) + pr[0], v1 = (kg ? c[3] : c[1]) + pr[64];
  *(u32x4*)(out + xo + kg * 32 + g * 8) = u32x4{pk2(v0[0], v0[1]), pk2(v0[2], v0[3]), pk2(v1[0], v1[1]), pk2(v1[2], v1[3])};
  if (!kg) { const f32x4 v2 = c[4] + pr[128]; *(u32x2*)(out + xo + 64 + g * 4) = u32x2{pk2(v2[0], v2[1]), pk2(v2[2], v2[3])}; }
}

__global__ __launch_bounds__(512, 4) void k2_attn_down(const u16* __restrict__ x, char* __restrict__ ws, u16* __restrict__ out) {
  __shared__ __attribute__((aligned(16))) char smem[K2_LDS];
  const int tid = threadIdx.x;
  const int wave = __builtin_amdgcn_readfirstlane(tid >> 6);
  const int b = blockIdx.x, xcd = b & 7, o = (b >> 3) & 31;
  PF_DECL; STAMP(1, 0);
  if (b < 256) {
    attn_role(smem, ws, xcd * 4 + (o >> 3), o & 7, tid, wave PF_PASS);
  } else {
#define X_SLEEP 64
    __builtin_amdgcn_s_sleep(X_SLEEP);
    down_role(smem, x, ws, out, (xcd & 3) * 8 + (o & 7), (xcd >> 2) * 4 + (o >> 3), tid, wave PF_PASS);
  }
  STAMP(1, 3);
}

extern "C" void kernel_launch(void* const* d_in, const int* in_sizes, int n_in, void* d_out, int out_size, void* d_ws, size_t ws_size,
                              hipStream_t stream) {
  const u16* x = (const u16*)d_in[0];
  char* ws = (char*)d_ws;
  k1_proj<<<256, 512, 0, stream>>>(x, (const u16*)d_in[1], (const u16*)d_in[4], (const u16*)d_in[5], (const u16*)d_in[2], (const u16*)d_in[6], ws);
  k2_attn_down<<<512, 512, 0, stream>>>(x, ws, (u16*)d_out);
}
```

```cpp
#include <hip/hip_runtime.h>
#include <hip/hip_bf16.h>

typedef unsigned short u16;
typedef __attribute__((ext_vector_type(8))) __bf16 bf16x8;
typedef __attribute__((ext_vector_type(2))) __bf16 bf16x2;
typedef __attribute__((ext_vector_type(4))) short s16x4;
typedef __attribute__((ext_vector_type(8))) short s16x8;
typedef __attribute__((ext_vector_type(16))) float f32x16;
typedef __attribute__((ext_vector_type(4))) float f32x4;
typedef __attribute__((ext_vector_type(2))) float f32x2;
typedef __attribute__((ext_vector_type(4))) unsigned u32x4;
typedef __attribute__((ext_vector_type(2))) unsigned u32x2;
#define LAS __attribute__((address_space(3)))
typedef LAS const char* lds_cptr;
typedef __attribute__((address_space(1))) unsigned gu32;

constexpr int S_ = 2048, D_ = 640, F_ = 1728, N1_ = 1920;
constexpr int QT = 1280;
constexpr int VB = 352;
constexpr int KVT = 1024 + 4 * VB;
constexpr int KVP = 2 * KVT + 512;
constexpr int KVH = 32 * KVP;
constexpr int KS3 = 74;
constexpr size_t WS_ST = 0;
constexpr size_t WS_Q = WS_ST + 16384;
constexpr size_t WS_KV = WS_Q + (size_t)32 * 64 * QT;
constexpr size_t WS_A3 = WS_KV + (size_t)32 * KVH;
constexpr size_t WS_W3 = WS_A3 + (size_t)32 * KS3 * 4096;
constexpr size_t WS_FLG = WS_W3 + (size_t)8 * KS3 * 5120;
constexpr int FQ = 64, FG = 512, FR = 1024, FO = 1152;

#define MFMA32(a, b, c) __builtin_amdgcn_mfma_f32_32x32x16_bf16(a, b, c, 0, 0, 0)
#define MFMA32X8(a, b, c) __builtin_amdgcn_mfma_f32_32x32x8bf16_1k(a, b, c, 0, 0, 0)
#define MFMA16(a, b, c) __builtin_amdgcn_mfma_f32_16x16x32_bf16(a, b, c, 0, 0, 0)
#define SGB(m, n) __builtin_amdgcn_sched_group_barrier(m, n, 0)
#define WSR __builtin_amdgcn_make_buffer_rsrc(ws, 0, (int)WS_FLG, 0x00020000)
#define ST16(p, v) __builtin_amdgcn_raw_buffer_store_b128(v, WSR, (int)((p) - ws), 0, 16)
#define ST8(p, v) __builtin_amdgcn_raw_buffer_store_b64(v, WSR, (int)((p) - ws), 0, 16)
#define LD16(vo, so) __builtin_bit_cast(bf16x8, __builtin_amdgcn_raw_buffer_load_b128(WSR, vo, so, 16))
#define LD8(vo, so) __builtin_amdgcn_raw_buffer_load_b64(WSR, vo, so, 16)
#define RLX __ATOMIC_RELAXED, __HIP_MEMORY_SCOPE_AGENT
#define SBAR() __builtin_amdgcn_sched_barrier(0)
#define PIN(x) asm volatile("" : "+v"(x))
#define WAIT_BAR(N) asm volatile("s_waitcnt vmcnt(" #N ") lgkmcnt(0)\n\ts_barrier" ::: "memory")
#define LGKM_BAR() asm volatile("s_waitcnt lgkmcnt(0)\n\ts_barrier" ::: "memory")

#define X_PH 2
#define X_POLL 4
#define X_STG 4
#define X_PB1 1
#define X_PB2 0
#define X_PB3 0
#define X_BS 0
#define X_PF 1
#define X_LM 0
#define XL(b) if (!(X_LM & b))
#define PF_DECL
#define PF_ARG
#define PF_PASS
#define STAMP(k, i)
__device__ __forceinline__ void glds16(const void* g, unsigned lds_base) {
  asm volatile("s_mov_b32 m0, %1\n\ts_nop 0\n\tglobal_load_lds_dwordx4 %0, off" :: "v"(g), "s"(lds_base) : "memory", "m0");
}
__device__ __forceinline__ void glds16s(const void* base, unsigned off, unsigned lds_base) {
  asm volatile("s_mov_b32 m0, %2\n\ts_nop 0\n\tglobal_load_lds_dwordx4 %0, %1" :: "v"(off), "s"(base), "s"(lds_base) : "memory", "m0");
}
__device__ __forceinline__ void glds16d(const void* g, unsigned lds_base) {
  asm volatile("s_mov_b32 m0, %1\n\ts_nop 0\n\tglobal_load_lds_dwordx4 %0, off sc1" :: "v"(g), "s"(lds_base) : "memory", "m0");
}
__device__ __forceinline__ void publish(char* ws, int slot, unsigned tag, int tid) {
  WAIT_BAR(0);
  if (tid == 0) __hip_atomic_store((gu32*)(ws + WS_FLG) + slot, tag, RLX);
}
__device__ __forceinline__ void poll(char* ws, int slot, unsigned tag) {
  for (int i = 0; i < (1 << 18); i++) {
    if (__all(__hip_atomic_load((gu32*)(ws + WS_FLG) + slot, RLX) == tag)) break;
    __builtin_amdgcn_s_sleep(X_POLL);
  }
}
__device__ __forceinline__ unsigned pk2(float lo, float hi) {
  f32x2 v = {lo, hi};
  return __builtin_bit_cast(unsigned, __builtin_convertvector(v, bf16x2));
}
__device__ __forceinline__ float bfr(float v) { return (float)(__bf16)v; }
__device__ __forceinline__ float bflo(unsigned u) { return __uint_as_float(u << 16); }
__device__ __forceinline__ float bfhi(unsigned u) { return __uint_as_float(u & 0xffff0000u); }
__device__ __forceinline__ bf16x8 lds128(lds_cptr p) { return *(const LAS bf16x8*)p; }
__device__ __forceinline__ s16x4 lds64(lds_cptr p) { return *(const LAS s16x4*)p; }
__device__ __forceinline__ bf16x8 ldstr2(lds_cptr p, int second) {
  s16x4 a = __builtin_amdgcn_ds_read_tr16_b64_v4i16((LAS s16x4*)p);
  s16x4 b = __builtin_amdgcn_ds_read_tr16_b64_v4i16((LAS s16x4*)(p + second));
  return __builtin_bit_cast(bf16x8, __builtin_shufflevector(a, b, 0, 1, 2, 3, 4, 5, 6, 7));
}
__device__ __forceinline__ float xhalf_max(float v) {
  auto r = __builtin_amdgcn_permlane32_swap(__float_as_uint(v), __float_as_uint(v), false, false);
  return fmaxf(__uint_as_float(r[0]), __uint_as_float(r[1]));
}
__device__ __forceinline__ void ln_stats(float sum, float ssq, float& mean_b, float& std_b) {
  float mean = sum * (1.0f / D_);
  float var = fmaxf(ssq * (1.0f / D_) - mean * mean, 0.0f);
  mean_b = bfr(mean);
  std_b = bfr(sqrtf(bfr(bfr(var) + 1e-5f)));
}

constexpr int STG = 12288, X0 = 4 * STG;
#define WAIT_BARN(N) asm volatile("s_waitcnt vmcnt(%0) lgkmcnt(0)\n\ts_barrier" :: "n"(N) : "memory")

template <int MODE>
struct K1Core {
  static constexpr int NB = MODE == 2 ? 6 : 5;
  f32x4 acc[NB][2], cs[2], rs, sq;
  bf16x8 sel;
  const u16 *xs, *wpa, *wpb;
  unsigned xl, wla, wlb;
  int wstep, xo, wo[5], wave;
  unsigned lds0;
  lds_cptr lb;
  static constexpr int ROW = MODE == 2 ? 192 : 320;
  __device__ __forceinline__ void issue_x(int h) {
    const unsigned dst = lds0 + X0 + (h & 2) * 8192 + wave * 2048;
    glds16s(xs + h * 32, xl, dst); glds16s(xs + h * 32 + 8 * D_, xl ^ 64, dst + 1024);
  }
  template <int PAR, int WM> __device__ __forceinline__ void issue_w(int h) {
    const unsigned dst = lds0 + (h & 3) * STG;
    glds16s(wpa + h * wstep, wla, dst + wave * 1024);
    if (MODE == 2 ? (WM >> 1) == PAR : WM == 2 * PAR) glds16s(wpb + h * wstep, wlb, dst + (8 + (wave & 3)) * 1024);
  }
  template <int PAR, int WM> __device__ __forceinline__ void step(int h, bool more) {
    int so = (h & 3) * STG, sx = (h & 2) * 8192;
    asm volatile("" : "+s"(so), "+s"(sx));
    lds_cptr st = lb + so;
    bf16x8 xf[2], wf[NB];
    xf[0] = lds128(lb + sx + (xo ^ (PAR * 64))); xf[1] = lds128(lb + sx + (xo ^ (PAR * 64) ^ 2048));
#pragma unroll
    for (int j = 0; j < NB; j++) wf[j] = ldstr2(st + (MODE == 2 ? wo[j % 3] + (j / 3) * 6144 : wo[j]), 4 * ROW);
    SBAR();
    if (more && (WM < X_STG)) { if (!PAR) issue_x(h + 2); issue_w<PAR ^ 1, WM>(h + 3); }
    SBAR();
    rs = MFMA16(sel, xf[0], rs);
    if (MODE != 2) sq = MFMA16(xf[0], xf[0], sq);
#pragma unroll
    for (int j = 0; j < NB; j++) {
#pragma unroll
      for (int a = 0; a < 2; a++) acc[j][a] = MODE == 1 ? MFMA16(xf[a], wf[j], acc[j][a]) : MFMA16(wf[j], xf[a], acc[j][a]);
      if ((j & 3) == WM) cs[j >> 2] = MFMA16(sel, wf[j], cs[j >> 2]);
    }
    SBAR();
#pragma unroll
    for (int j = 0; j < NB; j++) asm volatile("" : "+v"(acc[j][0]), "+v"(acc[j][1]));
    asm volatile("" : "+v"(cs[0]), "+v"(rs));
    if (WM + 4 < NB) PIN(cs[1]);
    if (MODE != 2) PIN(sq);
    if (more && !(WM < X_STG)) { if (!PAR) issue_x(h + 2); issue_w<PAR ^ 1, WM>(h + 3); }
  }
  template <int WM> __device__ __forceinline__ void run(char* ws PF_ARG) {
    constexpr int W2 = (MODE == 2 || !(WM & 1)) ? 3 : 2;
    issue_w<0, WM>(0); issue_x(0); issue_w<1, WM>(1); issue_w<0, WM>(2);
#pragma unroll
    for (int j = 0; j < NB; j++) acc[j][0] = acc[j][1] = f32x4{};
    cs[0] = cs[1] = rs = sq = f32x4{};
    for (int h = 0; h < 18; h += 2) {
      WAIT_BARN(W2); step<0, WM>(h, true);
      WAIT_BARN(W2 + 2); step<1, WM>(h + 1, h < 16);
    }
    WAIT_BAR(0); step<0, WM>(18, false); step<1, WM>(19, false);
  }
};

template <int MODE>
__device__ __forceinline__ void k1_tile(char* smem, const u16* __restrict__ x, const u16* __restrict__ w0, const u16* __restrict__ w1,
                                        char* __restrict__ ws, int rb, int ct, int tid, int wave PF_ARG) {
  constexpr int NB = K1Core<MODE>::NB, LD = MODE == 2 ? F_ : N1_;
  const int lane = tid & 63, i = lane & 15, g = lane >> 4, wm = wave >> 1, wn = wave & 1;
  K1Core<MODE> c;
  c.wave = wave; c.lds0 = (unsigned)(uintptr_t)smem; c.lb = (lds_cptr)smem; c.wstep = 32 * LD;
  c.xs = x + (rb * 128 + wave * 16) * D_;
  c.xl = (lane >> 3) * D_ * 2 + (((lane & 7) ^ g) << 4);
  {
    constexpr int CPR = K1Core<MODE>::ROW / 16;
    const int pa = MODE == 2 && wave >= 6 ? wave - 6 : wave, pb = MODE == 2 ? 2 + (wave & 3) : 8 + (wave & 1);
    const int ca = pa * 64 + lane, ra = ca / CPR, cb = pb * 64 + lane, rc = cb / CPR;
    c.wla = ra * LD * 2 + (((ca - ra * CPR) ^ ((ra >> 2) & 2)) << 4);
    c.wlb = rc * LD * 2 + (((cb - rc * CPR) ^ ((rc >> 2) & 2)) << 4);
    c.wpa = (wave >= 6 ? w1 : w0) + ct * (CPR * 8);
    c.wpb = w1 + ct * (CPR * 8);
  }
  c.xo = X0 + wave * 2048 + i * 128 + ((g ^ (i >> 1)) << 4);
  {
    const int q = i >> 2, p = lane & 3;
#pragma unroll
    for (int j = 0; j < 5; j++) {
      const int Q = MODE == 0 ? 5 * p + j + 20 * wn : MODE == 1 ? 4 * j + p + 20 * wn : 4 * (wn ? (j == 2 ? 3 : 4 + j) : j) + p;
      c.wo[j] = (8 * g + q) * K1Core<MODE>::ROW + ((Q ^ (4 * (g & 1))) << 3);
    }
    const unsigned v = i == 0 ? 0x3f803f80u : 0u;
    c.sel = __builtin_bit_cast(bf16x8, u32x4{v, v, v, v});
  }
  if (wm == 0) c.template run<0>(ws PF_PASS);
  else if (wm == 1) c.template run<1>(ws PF_PASS);
  else if (wm == 2) c.template run<2>(ws PF_PASS);
  else c.template run<3>(ws PF_PASS);
  STAMP(0, 2);
  float* sc = (float*)smem;
  if (g == 0) {
    sc[wave * 16 + i] = c.rs[0];
    sc[1792 + wn * 96 + wm * 16 + i] = c.cs[0][0];
    if (wm + 4 < NB) sc[1792 + wn * 96 + (wm + 4) * 16 + i] = c.cs[1][0];
  }
  if (MODE != 2 && g == (i >> 2)) {
    const float s01 = (i & 1) ? c.sq[1] : c.sq[0], s23 = (i & 1) ? c.sq[3] : c.sq[2];
    sc[256 + wave * 16 + i] = (i & 2) ? s23 : s01;
  }
  LGKM_BAR();
  if (tid < 128) {
    if (MODE == 2) {
      sc[1536 + tid] = -bfr(sc[tid] * (1.0f / D_));
    } else {
      float mean_b, std_b;
      ln_stats(sc[tid], sc[256 + tid], mean_b, std_b);
      const float r = 1.0f / std_b;
      sc[1280 + tid] = r; sc[1536 + tid] = -mean_b * r;
      if (ct == 0) __hip_atomic_store((gu32*)(ws + WS_ST) + rb * 128 + tid, __float_as_uint(r * r), RLX);
    }
  }
  LGKM_BAR();
  if (MODE == 0) {
    const bool isq = ct < 4;
    const float scl = isq ? 0.22360679774997896f * 1.4426950408889634f : 1.0f;
    const int head = (ct & 3) * 8 + wn * 4 + g;
    f32x4 cv[5];
#pragma unroll
    for (int j = 0; j < 5; j++) cv[j] = *(const f32x4*)(sc + 1792 + wn * 96 + j * 16 + 4 * g);
#pragma unroll
    for (int a = 0; a < 2; a++) {
      const int blk = a ^ wn, row = wm * 32 + blk * 16 + i, odd = wm & 1;
      const float r = sc[1280 + row] * scl, m = sc[1536 + row] * scl;
      unsigned w[10];
#pragma unroll
      for (int j = 0; j < 5; j++) {
        w[2 * j] = pk2(c.acc[j][a][0] * r + m * cv[j][0], c.acc[j][a][1] * r + m * cv[j][1]);
        w[2 * j + 1] = pk2(c.acc[j][a][2] * r + m * cv[j][2], c.acc[j][a][3] * r + m * cv[j][3]);
      }
      const int rr = isq ? blk * 16 + i : 16 * (i >> 3) + 8 * blk + (i & 7);
      char* tb = isq ? ws + WS_Q + (head * 64 + rb * 4 + wm) * QT : ws + WS_KV + head * KVH + (rb * 2 + (wm >> 1)) * KVP + odd * KVT;
      ST16(tb + rr * 16, (u32x4{w[0], w[1], w[2], w[3]}));
      ST16(tb + 512 + rr * 16, (u32x4{w[4], w[5], w[6], w[7]}));
      ST8(tb + (isq ? 1024 + rr * 8 : 2 * KVT - odd * (KVT - 8) + rr * 16), (u32x2{w[8], w[9]}));
    }
  } else if (MODE == 1) {
    {
      const int odd = wm & 1, row = wm * 32 + 4 * g;
      const f32x4 r0 = *(const f32x4*)(sc + 1280 + row), r1 = *(const f32x4*)(sc + 1296 + row);
      const f32x4 m0 = *(const f32x4*)(sc + 1536 + row), m1 = *(const f32x4*)(sc + 1552 + row);
#pragma unroll
      for (int j = 0; j < 5; j++) {
        const int n = 16 * j + i, hl = (n * 52429) >> 20, d = n - hl * 20;
        const float cv = sc[1792 + wn * 96 + n];
        const f32x4 u = (wn ? c.acc[j][1] : c.acc[j][0]) * r0 + m0 * cv, v = (wn ? c.acc[j][0] : c.acc[j][1]) * r1 + m1 * cv;
        char* tb = smem + 8192 + wave * 5632 + hl * 1408 + g * VB + d * 16;
        *(u32x4*)tb = u32x4{pk2(u[0], u[1]), pk2(u[2], u[3]), pk2(v[0], v[1]), pk2(v[2], v[3])};
        if (j > 0 && d >= 18) { const unsigned k = (d == 18) ? 0x3f803f80u : 0u; *(u32x4*)(tb + 32) = u32x4{k, k, k, k}; }
      }
      asm volatile("s_waitcnt lgkmcnt(0)" ::: "memory");
      char* vb = ws + WS_KV + ((ct & 3) * 8 + wn * 4) * KVH + (rb * 2 + (wm >> 1)) * KVP + odd * KVT + 1024;
#pragma unroll
      for (int e = 0; e < 6; e++) {
        const int n = e * 64 + lane, hl = n / 88;
        if (n < 352) ST16(vb + hl * (KVH - 1408) + n * 16, *(const u32x4*)(smem + 8192 + wave * 5632 + n * 16));
      }
    }
  } else {
    f32x4 cv[6];
#pragma unroll
    for (int j = 0; j < 6; j++) cv[j] = *(const f32x4*)(sc + 1792 + wn * 96 + j * 16 + 4 * g);
#pragma unroll
    for (int a = 0; a < 2; a++) {
      const int sub = (wm & 1) * 2 + (a ^ wn);
      const float m = sc[1536 + wm * 32 + (a ^ wn) * 16 + i];
      unsigned w[6];
#pragma unroll
      for (int j = 0; j < 3; j++)
#pragma unroll
        for (int e = 0; e < 2; e++) {
          const unsigned gp = pk2(c.acc[j][a][2 * e] + m * cv[j][2 * e], c.acc[j][a][2 * e + 1] + m * cv[j][2 * e + 1]);
          const unsigned up = pk2(c.acc[j + 3][a][2 * e] + m * cv[j + 3][2 * e], c.acc[j + 3][a][2 * e + 1] + m * cv[j + 3][2 * e + 1]);
          w[2 * j + e] = pk2(bflo(gp) * bflo(up), bfhi(gp) * bfhi(up));
        }
      char* ab = ws + WS_A3 + (((rb * 2 + (wm >> 1)) * KS3 + 20 + ct * 3) * 4 + sub) * 1024;
      ST16(ab + wn * 8192 + lane * 16, (u32x4{w[0], w[1], w[2], w[3]}));
      ST8(ab + 4096 + wn * 512 + (g >> 1) * 256 + i * 16 + (g & 1) * 8, (u32x2{w[4], w[5]}));
    }
  }
}

__device__ __forceinline__ void retile_role(char* smem, const u16* __restrict__ wo, const u16* __restrict__ wd, char* __restrict__ ws,
                                            int idx, int tid, int wave) {
  const int lane = tid & 63, g = lane >> 4, q = (lane & 15) >> 2, p = lane & 3;
  const unsigned lds0 = (unsigned)(uintptr_t)smem;
  for (int k0 = idx; k0 < KS3; k0 += 64) {
    WAIT_BAR(0);
    for (int u = 0; u < 2; u++) {
      const int ks = k0 + 32 * u;
      if (ks < KS3) {
        const int orow = ks < 16 ? (2 * ks + (wave >> 2)) * 20 + (wave & 2) * 2 + (wave & 1) * 8 : (8 * (ks - 16) + wave) * 20 + 16;
        const char* src = (const char*)(ks < 20 ? wo + (size_t)orow * D_ : wd + (size_t)((ks - 20) * 32 + wave * 4) * D_) + lane * 16;
#pragma unroll
        for (int e = 0; e < 5; e++) glds16(src + e * 1024, lds0 + u * 40960 + wave * 5120 + e * 1024);
      }
    }
    WAIT_BAR(0);
    for (int u = 0; u < 2; u++) {
      const int ks = k0 + 32 * u;
      if (ks < KS3) {
        const bool nat = ks < 20 || ks % 3 == 0;
        const int row = nat ? 8 * g + q : 4 * g + q, sec = (nat ? 4 : 16) * 1280;
        lds_cptr b = (lds_cptr)smem + u * 40960 + row * 1280 + wave * 160;
#pragma unroll
        for (int t = 0; t < 5; t++)
          ST16(ws + WS_W3 + ((wave * KS3 + ks) * 5 + t) * 1024 + lane * 16, __builtin_bit_cast(u32x4, ldstr2(b + (t < 4 ? (t >> 1) * 64 + p * 16 + (t & 1) * 8 : 128 + p * 8), sec)));
      }
    }
  }
}

constexpr int AT_BUF = 4 * KVP;
constexpr int K2_LDS = 81920;

#define TOFF(t) (((t) >> 1) * KVP + ((t) & 1) * KVT)
struct KFrag { bf16x8 k1; s16x4 k2; };

__device__ __forceinline__ f32x16 qk_tile(const KFrag& k, bf16x8 q1, s16x4 q2) {
  f32x16 z = {};
  f32x16 s = MFMA32(k.k1, q1, z);
  return MFMA32X8(k.k2, q2, s);
}
__device__ __forceinline__ bf16x8 exp_pack(const f32x16& s, int m) {
  u32x4 w;
#pragma unroll
  for (int i = 0; i < 4; i++) w[i] = pk2(__builtin_amdgcn_exp2f(s[8 * m + 2 * i]), __builtin_amdgcn_exp2f(s[8 * m + 2 * i + 1]));
  return __builtin_bit_cast(bf16x8, w);
}
__device__ __forceinline__ float max16(const f32x16& s) {
  float a = fmaxf(s[0], s[1]);
#pragma unroll
  for (int i = 2; i < 16; i += 2) a = fmaxf(fmaxf(a, s[i]), s[i + 1]);
  return a;
}

#define AT_GROUP(MF, ACC, A, B, P, PJ, SJ) do { unsigned pa_, pb_; SBAR();                                                        \
    asm volatile(MF "\n\tv_cvt_pk_bf16_f32 %1, %3, %4\n\tv_cvt_pk_bf16_f32 %2, %5, %6\n\tv_exp_f32 %3, %9\n\tv_exp_f32 %4, %10\n\t"    \
                 "v_exp_f32 %5, %11\n\tv_exp_f32 %6, %12"                                                                          \
                 : ACC, "=&v"(pa_), "=&v"(pb_), "+v"(e[0]), "+v"(e[1]), "+v"(e[2]), "+v"(e[3])                                     \
                 : "v"(A), "v"(B), "v"(sa[SJ]), "v"(sa[SJ + 1]), "v"(sa[SJ + 2]), "v"(sa[SJ + 3]));                                \
    P[PJ] = pa_; P[PJ + 1] = pb_; SBAR(); } while (0)

__device__ __forceinline__ void attn_role(char* smem, char* __restrict__ ws, unsigned tag, int head, int qb, int tid, int wave PF_ARG) {
  const int lane = tid & 63, r = lane & 31, hh = lane >> 5;
  const unsigned lds0 = (unsigned)(uintptr_t)smem;
  const char* kvsrc = ws + WS_KV + (size_t)head * KVH + wave * 1024 + lane * 16;
  auto dma = [&](int c, int buf) {
    const char* src = kvsrc + (size_t)c * AT_BUF;
    const unsigned dst = lds0 + buf + wave * 1024;
    glds16d(src, dst);
    glds16d(src + 8192, dst + 8192);
    if (wave < 5) glds16d(src + 16384, dst + 16384);
  };
  if (wave == 0) poll(ws, FQ + (head >> 3) * 64 + min(lane, 47), tag);
  LGKM_BAR();
  STAMP(1, 0);
  dma(0, 0);
  const int qo = (int)WS_Q + (head * 64 + qb * 8 + wave) * QT;
  const bf16x8 q1 = LD16(lane * 16, qo);
  u32x2 q2u = LD8(1024 + r * 8, qo);
  if (hh) q2u = u32x2{0u, 0u};
  const int k1o = lane * 16, k2o = hh ? 1024 + 320 : 2 * KVT + r * 16, vo = 1024 + hh * VB + min(r, 21) * 16;
  lds_cptr lb = (lds_cptr)smem;
  LAS int* flag = (LAS int*)(lb + 3 * AT_BUF + 512);
  auto ldk = [&](int t) { KFrag k; k.k1 = lds128(lb + TOFF(t) + k1o); k.k2 = lds64(lb + (t >> 1) * KVP + (t & 1) * 8 + k2o); return k; };
  auto set_ref = [&](float m) {
    const float m0 = bfr(m), m1 = bfr(m - m0), m2 = bfr(m - m0 - m1);
    if (hh) q2u = u32x2{pk2(-m0, -m1), pk2(-m2, 0.f)};
  };
  WAIT_BAR(0);
  dma(1, AT_BUF);
  {
    KFrag k = ldk(0);
    set_ref(xhalf_max(max16(qk_tile(k, q1, __builtin_bit_cast(s16x4, q2u)))));
  }
  f32x16 o;
  STAMP(1, 1);
  for (int attempt = 0;; attempt++) {
    const s16x4 q2 = __builtin_bit_cast(s16x4, q2u);
    o = f32x16{};
    f32x16 sa;
    { KFrag k = ldk(0); sa = qk_tile(k, q1, q2); }
    bf16x8 k1 = lds128(lb + KVT + k1o), v1 = {}, v0;
    s16x8 k2p = __builtin_bit_cast(s16x8, lds128(lb + k2o));
    v0 = lds128(lb + vo);
    u32x4 p0 = {0u, 0u, 0u, 0u}, p1 = {0u, 0u, 0u, 0u};
    float e[4] = {0.f, 0.f, 0.f, 0.f};
    int cur = 0, nxt = AT_BUF;
    asm volatile("s_nop 15\n\ts_nop 7" : "+v"(sa), "+v"(o), "+v"(p1), "+v"(v1));
    for (int c = 0; c < 8; c++) {
      lds_cptr ck1 = lb + cur + k1o, ck2 = lb + cur + k2o, cv = lb + cur + vo;
      lds_cptr nk1 = lb + nxt + k1o, nk2 = lb + nxt + k2o, nv = lb + nxt + vo;
      const int nn = (nxt == 2 * AT_BUF) ? 0 : nxt + AT_BUF;
#pragma unroll
      for (int t = 0; t < 8; t++) {
        XL(8) if (t == 6) asm volatile("s_waitcnt vmcnt(0)\n\ts_barrier" ::: "memory");
        const int pc = (t + 2) & 7;
        const bool ld = pc < 3 && (pc < 2 ? c < 6 : (c > 0 && c < 7 && wave < 5));
        const char* src = kvsrc + (size_t)(t == 0 ? c + 1 : c + 2) * AT_BUF + pc * 8192;
        const unsigned dst = lds0 + (t == 0 ? nxt : nn) + wave * 1024 + pc * 8192;
        XL(8) if (ld && wave < 4) glds16d(src, dst);
        f32x16 sb;
        const s16x4 k2 = (t & 1) ? __builtin_shufflevector(k2p, k2p, 0, 1, 2, 3) : __builtin_shufflevector(k2p, k2p, 4, 5, 6, 7);
        AT_GROUP("v_mfma_f32_32x32x16_bf16 %0, %7, %8, 0", "=&v"(sb), k1, q1, p1, 2, 0);
        XL(1) k1 = lds128((t < 6 ? ck1 : nk1) + TOFF((t + 2) & 7));
        AT_GROUP("v_mfma_f32_32x32x16_bf16 %0, %7, %8, %0", "+v"(o), v1, p1, p0, 0, 4);
        XL(2) v1 = lds128(cv + TOFF(t) + 2 * VB);
        XL(8) if (ld && wave >= 4) glds16d(src, dst);
        AT_GROUP("v_mfma_f32_32x32x8_bf16 %0, %7, %8, %0", "+v"(sb), k2, q2, p0, 2, 8);
        XL(4) if (!(t & 1)) k2p = __builtin_bit_cast(s16x8, lds128((t < 6 ? ck2 : nk2) + TOFF((t + 2) & 7)));
        AT_GROUP("v_mfma_f32_32x32x16_bf16 %0, %7, %8, %0", "+v"(o), v0, p0, p1, 0, 12);
        XL(2) v0 = lds128((t < 7 ? cv : nv) + TOFF((t + 1) & 7));
        sa = sb;
      }
      cur = nxt; nxt = nn;
    }
    asm volatile("s_nop 7" : "+v"(o), "+v"(e[3]));
    p1[2] = pk2(e[0], e[1]); p1[3] = pk2(e[2], e[3]);
    o = MFMA32(v1, __builtin_bit_cast(bf16x8, p1), o);
    if (attempt == 1) break;
    bool ok = true;
#pragma unroll
    for (int i = 0; i < 12; i++) ok = ok && (fabsf(o[i]) < 1e30f || (hh && i > 8));
    LGKM_BAR();
    if (tid == 0) *flag = 0;
    LGKM_BAR();
    if (!ok) *flag = 1;
    LGKM_BAR();
    if (*flag == 0) break;
    if (hh) q2u = u32x2{0u, 0u};
    const s16x4 qz = __builtin_bit_cast(s16x4, q2u);
    float mx = -INFINITY;
    for (int c = 0; c < 8; c++) {
      LGKM_BAR();
      dma(c, 0);
      WAIT_BAR(0);
      for (int t = 0; t < 8; t++) { KFrag k = ldk(t); mx = fmaxf(mx, max16(qk_tile(k, q1, qz))); }
    }
    set_ref(xhalf_max(mx));
    LGKM_BAR();
    dma(0, 0); dma(1, AT_BUF);
    WAIT_BAR(0);
  }
  STAMP(1, 2);
  auto sw = __builtin_amdgcn_permlane32_swap(__float_as_uint(o[8]), __float_as_uint(o[8]), false, false);
  const float inv = 1.0f / __uint_as_float(sw[1]);
  const int s = qb * 256 + wave * 32 + r;
  char* ab = ws + WS_A3 + ((s >> 6) * KS3 * 4 + ((s >> 4) & 3)) * 1024;
  ST16(ab + (head >> 1) * 4096 + ((head & 1) * 2 + hh) * 256 + (s & 15) * 16,
       (u32x4{pk2(o[0] * inv, o[1] * inv), pk2(o[2] * inv, o[3] * inv), pk2(o[4] * inv, o[5] * inv), pk2(o[6] * inv, o[7] * inv)}));
  if (!hh) ST8(ab + (16 + (head >> 3)) * 4096 + (head & 7) * 128 + (s & 15) * 8, (u32x2{pk2(o[8] * inv, o[9] * inv), pk2(o[10] * inv, o[11] * inv)}));
  publish(ws, FO + qb * 32 + head, tag, tid);
}

__device__ __forceinline__ void down_role(char* smem, const u16* __restrict__ x, char* __restrict__ ws, u16* __restrict__ out, unsigned tag, int rb, int nb, int tid, int wave PF_ARG) {
  const int lane = tid & 63;
  if (wave == 0) poll(ws, lane < 18 ? FG + (rb >> 1) * 32 + lane : lane < 32 ? FQ + (rb >> 1) : FR - 32 + lane, tag);
  LGKM_BAR();
  STAMP(1, 0);
  int ap = (int)WS_A3 + (rb * KS3 + 20 + wave) * 4096, wp = (int)WS_W3 + (nb * KS3 + 20 + wave) * 5120;
  bf16x8 a[4], w[5];
#pragma unroll
  for (int t = 0; t < 5; t++) w[t] = LD16(lane * 16 + (t & 3) * 1024, wp + (t >> 2) * 4096);
#pragma unroll
  for (int i = 0; i < 4; i++) a[i] = LD16(lane * 16 + i * 1024, ap);
  f32x4 acc[20] = {};
  const int n = wave < 6 ? 7 : 6;
  for (int it = 1; it <= n; it++) {
    if (it < n) { ap += 8 * 4096; wp += 8 * 5120; }
#pragma unroll
    for (int i = 0; i < 4; i++) {
#pragma unroll
      for (int t = 0; t < 5; t++) acc[t * 4 + i] = MFMA16(w[t], a[i], acc[t * 4 + i]);
      SBAR();
      a[i] = LD16(lane * 16 + i * 1024, ap);
    }
#pragma unroll
    for (int t = 0; t < 5; t++) w[t] = LD16(lane * 16 + (t & 3) * 1024, wp + (t >> 2) * 4096);
  }
  STAMP(1, 2);
  __builtin_amdgcn_s_setprio(X_PB3);
  f32x4* red = (f32x4*)smem;
  const int rbk = wave & 3, kg = wave >> 2, g = lane >> 4, row = rb * 64 + rbk * 16 + (lane & 15);
  const size_t xo = (size_t)row * D_ + nb * 80;
  const u32x4 xv = *(const u32x4*)(x + xo + kg * 32 + g * 8);
  const u32x2 xw = *(const u32x2*)(x + xo + 64 + g * 4);
  const float r2 = __uint_as_float(__hip_atomic_load((gu32*)(ws + WS_ST) + row, RLX));
  f32x4 fin[3];
#pragma unroll
  for (int rd = 0; rd < 3; rd++) {
    if (rd) LGKM_BAR();
#pragma unroll
    for (int u = 0; u < (rd < 2 ? 8 : 4); u++) red[(wave * 8 + u) * 64 + lane] = acc[(rd < 2 ? 2 * (u >> 2) + rd : 4) * 4 + (u & 3)];
    LGKM_BAR();
    f32x4 v = red[(rd < 2 ? wave : rbk) * 64 + lane];
#pragma unroll
    for (int k = 1; k < 8; k++) v += red[(k * 8 + (rd < 2 ? wave : rbk)) * 64 + lane];
    const unsigned x0 = rd < 2 ? xv[2 * rd] : xw[0], x1 = rd < 2 ? xv[2 * rd + 1] : xw[1];
    fin[rd] = v * r2 + f32x4{bflo(x0), bfhi(x0), bflo(x1), bfhi(x1)};
    PIN(fin[rd]);
  }
  LGKM_BAR();
  STAMP(1, 4);
  const char* wo = ws + WS_W3 + ((size_t)nb * KS3 + kg * 10) * 5120 + lane * 16;
  const unsigned lds0 = (unsigned)(uintptr_t)smem;
#pragma unroll
  for (int e = 0; e < 10; e++) glds16d(wo + (10 + rbk * 10 + e) * 1024, lds0 + (wave * 10 + e) * 1024);
  bf16x8 wr[10];
#pragma unroll
  for (int e = 0; e < 10; e++) wr[e] = LD16(lane * 16 + (e & 3) * 1024, (int)WS_W3 + (nb * KS3 + kg * 10) * 5120 + (e >> 2) * 4096);
  f32x4 c[5];
  c[0] = kg ? f32x4{} : fin[0]; c[1] = kg ? f32x4{} : fin[1]; c[2] = kg ? fin[0] : f32x4{}; c[3] = kg ? fin[1] : f32x4{}; c[4] = kg ? f32x4{} : fin[2];
#pragma unroll
  for (int e = 0; e < 10; e++) PIN(wr[e]);
  asm volatile("" : "+v"(c[0]), "+v"(c[1]), "+v"(c[2]), "+v"(c[3]), "+v"(c[4]));
  STAMP(1, 5);
  if (wave == 0) poll(ws, FO + (rb >> 2) * 32 + (lane & 31), tag);
  WAIT_BAR(0);
  STAMP(1, 6);
  const int ob = (int)WS_A3 + ((rb * KS3 + kg * 10) * 4 + rbk) * 1024, o2 = kg ? g * 256 + (lane & 15) * 8 : lane * 16, o3 = o2 + (kg ? 128 : 8);
  bf16x8 of[10];
#pragma unroll
  for (int k = 0; k < 10; k++) {
    if (k < 6) of[k] = LD16(lane * 16, ob + k * 4096);
    else {
      const u32x2 lo = LD8(o2, ob + k * 4096), hi = LD8(o3, ob + k * 4096);
      of[k] = __builtin_bit_cast(bf16x8, u32x4{lo[0], lo[1], hi[0], hi[1]});
    }
  }
  SBAR();
  lds_cptr lw = (lds_cptr)smem + kg * 40960 + lane * 16;
#pragma unroll
  for (int k = 0; k < 10; k++) {
#pragma unroll
    for (int t = 0; t < 5; t++) c[t] = MFMA16(wr[(k & 1) * 5 + t], of[k], c[t]);
    asm volatile("" : "+v"(c[0]), "+v"(c[1]), "+v"(c[2]), "+v"(c[3]), "+v"(c[4]) :: "memory");
    if (k < 8)
#pragma unroll
      for (int t = 0; t < 5; t++) wr[(k & 1) * 5 + t] = lds128(lw + (k * 5 + t) * 1024);
    SBAR();
  }
  LGKM_BAR();
  red[(wave * 3) * 64 + lane] = kg ? c[0] : c[2];
  red[(wave * 3 + 1) * 64 + lane] = kg ? c[1] : c[3];
  if (kg) red[(wave * 3 + 2) * 64 + lane] = c[4];
  LGKM_BAR();
  const f32x4* pr = red + ((wave ^ 4) * 3) * 64 + lane;
  const f32x4 v0 = (kg ? c[2] : c[0]) + pr[0], v1 = (kg ? c[3] : c[1]) + pr[64];
  *(u32x4*)(out + xo + kg * 32 + g * 8) = u32x4{pk2(v0[0], v0[1]), pk2(v0[2], v0[3]), pk2(v1[0], v1[1]), pk2(v1[2], v1[3])};
  if (!kg) { const f32x4 v2 = c[4] + pr[128]; *(u32x2*)(out + xo + 64 + g * 4) = u32x2{pk2(v2[0], v2[1]), pk2(v2[2], v2[3])}; }
}

#define X_PRI1 2
#define X_PRI2 0
__global__ __launch_bounds__(512, 4) void fused_block(const u16* __restrict__ x, const u16* __restrict__ wqkv, const u16* __restrict__ wg,
                                                      const u16* __restrict__ wu, const u16* __restrict__ wo, const u16* __restrict__ wd,
                                                      char* __restrict__ ws, u16* __restrict__ out) {
  __shared__ __attribute__((aligned(16))) char smem[K2_LDS];
  const int tid = threadIdx.x;
  const int wave = __builtin_amdgcn_readfirstlane(tid >> 6);
  const int b = blockIdx.x, xcd = b & 7, o = (b >> 3) & 31, cg = xcd >> 1, t = o >> 3, rb = (xcd & 1) * 8 + (o & 7);
  PF_DECL; STAMP(0, 0);
  int slot;
  if (b >= 256) {
    const int u = min(tid - 320, 159);
    const char* pf = u < 0 ? (const char*)(x + (rb * 128 + t * 32) * D_) + tid * 128 : (const char*)(wqkv + (o + (u >> 3) * 32) * N1_ + cg * 480) + (u & 7) * 128;
    const unsigned dst = (unsigned)(uintptr_t)smem + 3 * STG + wave * 256;
    asm volatile("s_mov_b32 m0, %1\n\ts_nop 0\n\tglobal_load_lds_dword %0, off" :: "v"(pf), "s"(dst) : "memory", "m0");
  }
  if (b < 256) __builtin_amdgcn_s_setprio(X_PRI1); else {
    __builtin_amdgcn_s_setprio(X_PB1);
    for (int i = 0; i < X_BS; i++) __builtin_amdgcn_s_sleep(32);
  }
  if (b < 256 && t < 3) {
    const int ct = cg * 3 + t;
    if (ct < 8) k1_tile<0>(smem, x, wqkv, wqkv, ws, rb, ct, tid, wave PF_PASS);
    else k1_tile<1>(smem, x, wqkv, wqkv, ws, rb, ct, tid, wave PF_PASS);
    slot = FQ + (ct & 3) * 64 + (ct >> 2) * 16 + rb;
  } else if (b >= 256 || !(cg & 1)) {
    const int ct = (cg >> 1) * 9 + (b < 256 ? 4 : (cg & 1) * 5 + t);
    k1_tile<2>(smem, x, wg, wu, ws, rb, ct, tid, wave PF_PASS);
    slot = FG + rb * 32 + ct;
  } else {
    retile_role(smem, wo, wd, ws, (cg >> 1) * 16 + rb, tid, wave);
    slot = FR + (cg >> 1) * 16 + rb;
  }
  STAMP(0, 4);
  const unsigned tag = __builtin_amdgcn_readfirstlane(__hip_atomic_load((gu32*)(ws + WS_FLG), RLX)) + 1u;
  publish(ws, slot, tag, tid);
  STAMP(0, 3);
  if (b < 256) {
    __builtin_amdgcn_s_setprio(X_PRI2);
    attn_role(smem, ws, tag, xcd * 4 + (o >> 3), o & 7, tid, wave PF_PASS);
  } else {
    __builtin_amdgcn_s_setprio(X_PB2);
    down_role(smem, x, ws, out, tag, (xcd & 3) * 8 + (o & 7), (xcd >> 2) * 4 + (o >> 3), tid, wave PF_PASS);
    if (b == 256) {
      poll(ws, FG + (tid >> 5) * 32 + min(tid & 31, 17), tag);
      publish(ws, 0, tag, tid);
    }
  }
  STAMP(1, 3);
}

extern "C" void kernel_launch(void* const* d_in, const int* in_sizes, int n_in, void* d_out, int out_size, void* d_ws, size_t ws_size,
                              hipStream_t stream) {
  fused_block<<<512, 512, 0, stream>>>((const u16*)d_in[0], (const u16*)d_in[1], (const u16*)d_in[4], (const u16*)d_in[5], (const u16*)d_in[2],
                                       (const u16*)d_in[6], (char*)d_ws, (u16*)d_out);
}
```

```cpp
#include <hip/hip_runtime.h>
#include <hip/hip_bf16.h>

typedef unsigned short u16;
typedef __attribute__((ext_vector_type(8))) __bf16 bf16x8;
typedef __attribute__((ext_vector_type(2))) __bf16 bf16x2;
typedef __attribute__((ext_vector_type(4))) short s16x4;
typedef __attribute__((ext_vector_type(8))) short s16x8;
typedef __attribute__((ext_vector_type(16))) float f32x16;
typedef __attribute__((ext_vector_type(4))) float f32x4;
typedef __attribute__((ext_vector_type(2))) float f32x2;
typedef __attribute__((ext_vector_type(4))) unsigned u32x4;
typedef __attribute__((ext_vector_type(2))) unsigned u32x2;
#define LAS __attribute__((address_space(3)))
typedef LAS const char* lds_cptr;
typedef __attribute__((address_space(1))) unsigned gu32;

constexpr int S_ = 2048, D_ = 640, F_ = 1728, N1_ = 1920;
constexpr int QT = 1280;
constexpr int VB = 352;
constexpr int KVT = 1024 + 4 * VB;
constexpr int KVP = 2 * KVT + 512;
constexpr int KVH = 32 * KVP;
constexpr int KS3 = 74;
constexpr size_t WS_ST = 0;
constexpr size_t WS_Q = WS_ST + 16384;
constexpr size_t WS_KV = WS_Q + (size_t)32 * 64 * QT;
constexpr size_t WS_A3 = WS_KV + (size_t)32 * KVH;
constexpr size_t WS_W3 = WS_A3 + (size_t)32 * KS3 * 4096;
constexpr size_t WS_FLG = WS_W3 + (size_t)8 * KS3 * 5120;
constexpr int FQ = 64, FG = 512, FR = 1024, FO = 1152;

#define MFMA32(a, b, c) __builtin_amdgcn_mfma_f32_32x32x16_bf16(a, b, c, 0, 0, 0)
#define MFMA32X8(a, b, c) __builtin_amdgcn_mfma_f32_32x32x8bf16_1k(a, b, c, 0, 0, 0)
#define MFMA16(a, b, c) __builtin_amdgcn_mfma_f32_16x16x32_bf16(a, b, c, 0, 0, 0)
#define SGB(m, n) __builtin_amdgcn_sched_group_barrier(m, n, 0)
#define WSR __builtin_amdgcn_make_buffer_rsrc(ws, 0, (int)WS_FLG, 0x00020000)
#define ST16(p, v) __builtin_amdgcn_raw_buffer_store_b128(v, WSR, (int)((p) - ws), 0, 16)
#define ST8(p, v) __builtin_amdgcn_raw_buffer_store_b64(v, WSR, (int)((p) - ws), 0, 16)
#define LD16(vo, so) __builtin_bit_cast(bf16x8, __builtin_amdgcn_raw_buffer_load_b128(WSR, vo, so, 16))
#define LD8(vo, so) __builtin_amdgcn_raw_buffer_load_b64(WSR, vo, so, 16)
#define RLX __ATOMIC_RELAXED, __HIP_MEMORY_SCOPE_AGENT
#define SBAR() __builtin_amdgcn_sched_barrier(0)
#define PIN(x) asm volatile("" : "+v"(x))
#define WAIT_BAR(N) asm volatile("s_waitcnt vmcnt(" #N ") lgkmcnt(0)\n\ts_barrier" ::: "memory")
#define LGKM_BAR() asm volatile("s_waitcnt lgkmcnt(0)\n\ts_barrier" ::: "memory")

#define X_PH 2
#define X_POLL 4
#define X_STG 4
#define X_PB1 1
#define X_PB2 0
#define X_PB3 0
#define X_BS 0
#define X_PF 2
#define X_LM 0
#define XL(b) if (!(X_LM & b))
#define PF_DECL
#define PF_ARG
#define PF_PASS
#define STAMP(k, i)
__device__ __forceinline__ void glds16(const void* g, unsigned lds_base) {
  asm volatile("s_mov_b32 m0, %1\n\ts_nop 0\n\tglobal_load_lds_dwordx4 %0, off" :: "v"(g), "s"(lds_base) : "memory", "m0");
}
__device__ __forceinline__ void glds16s(const void* base, unsigned off, unsigned lds_base) {
  asm volatile("s_mov_b32 m0, %2\n\ts_nop 0\n\tglobal_load_lds_dwordx4 %0, %1" :: "v"(off), "s"(base), "s"(lds_base) : "memory", "m0");
}
__device__ __forceinline__ void glds16d(const void* g, unsigned lds_base) {
  asm volatile("s_mov_b32 m0, %1\n\ts_nop 0\n\tglobal_load_lds_dwordx4 %0, off sc1" :: "v"(g), "s"(lds_base) : "memory", "m0");
}
__device__ __forceinline__ void publish(char* ws, int slot, unsigned tag, int tid) {
  WAIT_BAR(0);
  if (tid == 0) __hip_atomic_store((gu32*)(ws + WS_FLG) + slot, tag, RLX);
}
__device__ __forceinline__ void poll(char* ws, int slot, unsigned tag) {
  for (int i = 0; i < (1 << 18); i++) {
    if (__all(__hip_atomic_load((gu32*)(ws + WS_FLG) + slot, RLX) == tag)) break;
    __builtin_amdgcn_s_sleep(X_POLL);
  }
}
__device__ __forceinline__ unsigned pk2(float lo, float hi) {
  f32x2 v = {lo, hi};
  return __builtin_bit_cast(unsigned, __builtin_convertvector(v, bf16x2));
}
__device__ __forceinline__ float bfr(float v) { return (float)(__bf16)v; }
__device__ __forceinline__ float bflo(unsigned u) { return __uint_as_float(u << 16); }
__device__ __forceinline__ float bfhi(unsigned u) { return __uint_as_float(u & 0xffff0000u); }
__device__ __forceinline__ bf16x8 lds128(lds_cptr p) { return *(const LAS bf16x8*)p; }
__device__ __forceinline__ s16x4 lds64(lds_cptr p) { return *(const LAS s16x4*)p; }
__device__ __forceinline__ bf16x8 ldstr2(lds_cptr p, int second) {
  s16x4 a = __builtin_amdgcn_ds_read_tr16_b64_v4i16((LAS s16x4*)p);
  s16x4 b = __builtin_amdgcn_ds_read_tr16_b64_v4i16((LAS s16x4*)(p + second));
  return __builtin_bit_cast(bf16x8, __builtin_shufflevector(a, b, 0, 1, 2, 3, 4, 5, 6, 7));
}
__device__ __forceinline__ float xhalf_max(float v) {
  auto r = __builtin_amdgcn_permlane32_swap(__float_as_uint(v), __float_as_uint(v), false, false);
  return fmaxf(__uint_as_float(r[0]), __uint_as_float(r[1]));
}
__device__ __forceinline__ void ln_stats(float sum, float ssq, float& mean_b, float& std_b) {
  float mean = sum * (1.0f / D_);
  float var = fmaxf(ssq * (1.0f / D_) - mean * mean, 0.0f);
  mean_b = bfr(mean);
  std_b = bfr(sqrtf(bfr(bfr(var) + 1e-5f)));
}

constexpr int STG = 12288, X0 = 4 * STG;
#define WAIT_BARN(N) asm volatile("s_waitcnt vmcnt(%0) lgkmcnt(0)\n\ts_barrier" :: "n"(N) : "memory")

template <int MODE>
struct K1Core {
  static constexpr int NB = MODE == 2 ? 6 : 5;
  f32x4 acc[NB][2], cs[2], rs, sq;
  bf16x8 sel;
  const u16 *xs, *wpa, *wpb;
  unsigned xl, wla, wlb;
  int wstep, xo, wo[5], wave;
  unsigned lds0;
  lds_cptr lb;
  static constexpr int ROW = MODE == 2 ? 192 : 320;
  __device__ __forceinline__ void issue_x(int h) {
    const unsigned dst = lds0 + X0 + (h & 2) * 8192 + wave * 2048;
    glds16s(xs + h * 32, xl, dst); glds16s(xs + h * 32 + 8 * D_, xl ^ 64, dst + 1024);
  }
  template <int PAR, int WM> __device__ __forceinline__ void issue_w(int h) {
    const unsigned dst = lds0 + (h & 3) * STG;
    glds16s(wpa + h * wstep, wla, dst + wave * 1024);
    if (MODE == 2 ? (WM >> 1) == PAR : WM == 2 * PAR) glds16s(wpb + h * wstep, wlb, dst + (8 + (wave & 3)) * 1024);
  }
  template <int PAR, int WM> __device__ __forceinline__ void step(int h, bool more) {
    int so = (h & 3) * STG, sx = (h & 2) * 8192;
    asm volatile("" : "+s"(so), "+s"(sx));
    lds_cptr st = lb + so;
    bf16x8 xf[2], wf[NB];
    xf[0] = lds128(lb + sx + (xo ^ (PAR * 64))); xf[1] = lds128(lb + sx + (xo ^ (PAR * 64) ^ 2048));
#pragma unroll
    for (int j = 0; j < NB; j++) wf[j] = ldstr2(st + (MODE == 2 ? wo[j % 3] + (j / 3) * 6144 : wo[j]), 4 * ROW);
    SBAR();
    if (more && (WM < X_STG)) { if (!PAR) issue_x(h + 2); issue_w<PAR ^ 1, WM>(h + 3); }
    SBAR();
    rs = MFMA16(sel, xf[0], rs);
    if (MODE != 2) sq = MFMA16(xf[0], xf[0], sq);
#pragma unroll
    for (int j = 0; j < NB; j++) {
#pragma unroll
      for (int a = 0; a < 2; a++) acc[j][a] = MODE == 1 ? MFMA16(xf[a], wf[j], acc[j][a]) : MFMA16(wf[j], xf[a], acc[j][a]);
      if ((j & 3) == WM) cs[j >> 2] = MFMA16(sel, wf[j], cs[j >> 2]);
    }
    SBAR();
#pragma unroll
    for (int j = 0; j < NB; j++) asm volatile("" : "+v"(acc[j][0]), "+v"(acc[j][1]));
    asm volatile("" : "+v"(cs[0]), "+v"(rs));
    if (WM + 4 < NB) PIN(cs[1]);
    if (MODE != 2) PIN(sq);
    if (more && !(WM < X_STG)) { if (!PAR) issue_x(h + 2); issue_w<PAR ^ 1, WM>(h + 3); }
  }
  template <int WM> __device__ __forceinline__ void run(char* ws PF_ARG) {
    constexpr int W2 = (MODE == 2 || !(WM & 1)) ? 3 : 2;
    issue_w<0, WM>(0); issue_x(0); issue_w<1, WM>(1); issue_w<0, WM>(2);
#pragma unroll
    for (int j = 0; j < NB; j++) acc[j][0] = acc[j][1] = f32x4{};
    cs[0] = cs[1] = rs = sq = f32x4{};
    for (int h = 0; h < 18; h += 2) {
      WAIT_BARN(W2); step<0, WM>(h, true);
      WAIT_BARN(W2 + 2); step<1, WM>(h + 1, h < 16);
    }
    WAIT_BAR(0); step<0, WM>(18, false); step<1, WM>(19, false);
  }
};

template <int MODE>
__device__ __forceinline__ void k1_tile(char* smem, const u16* __restrict__ x, const u16* __restrict__ w0, const u16* __restrict__ w1,
                                        char* __restrict__ ws, int rb, int ct, int tid, int wave PF_ARG) {
  constexpr int NB = K1Core<MODE>::NB, LD = MODE == 2 ? F_ : N1_;
  const int lane = tid & 63, i = lane & 15, g = lane >> 4, wm = wave >> 1, wn = wave & 1;
  K1Core<MODE> c;
  c.wave = wave; c.lds0 = (unsigned)(uintptr_t)smem; c.lb = (lds_cptr)smem; c.wstep = 32 * LD;
  c.xs = x + (rb * 128 + wave * 16) * D_;
  c.xl = (lane >> 3) * D_ * 2 + (((lane & 7) ^ g) << 4);
  {
    constexpr int CPR = K1Core<MODE>::ROW / 16;
    const int pa = MODE == 2 && wave >= 6 ? wave - 6 : wave, pb = MODE == 2 ? 2 + (wave & 3) : 8 + (wave & 1);
    const int ca = pa * 64 + lane, ra = ca / CPR, cb = pb * 64 + lane, rc = cb / CPR;
    c.wla = ra * LD * 2 + (((ca - ra * CPR) ^ ((ra >> 2) & 2)) << 4);
    c.wlb = rc * LD * 2 + (((cb - rc * CPR) ^ ((rc >> 2) & 2)) << 4);
    c.wpa = (wave >= 6 ? w1 : w0) + ct * (CPR * 8);
    c.wpb = w1 + ct * (CPR * 8);
  }
  c.xo = X0 + wave * 2048 + i * 128 + ((g ^ (i >> 1)) << 4);
  {
    const int q = i >> 2, p = lane & 3;
#pragma unroll
    for (int j = 0; j < 5; j++) {
      const int Q = MODE == 0 ? 5 * p + j + 20 * wn : MODE == 1 ? 4 * j + p + 20 * wn : 4 * (wn ? (j == 2 ? 3 : 4 + j) : j) + p;
      c.wo[j] = (8 * g + q) * K1Core<MODE>::ROW + ((Q ^ (4 * (g & 1))) << 3);
    }
    const unsigned v = i == 0 ? 0x3f803f80u : 0u;
    c.sel = __builtin_bit_cast(bf16x8, u32x4{v, v, v, v});
  }
  if (wm == 0) c.template run<0>(ws PF_PASS);
  else if (wm == 1) c.template run<1>(ws PF_PASS);
  else if (wm == 2) c.template run<2>(ws PF_PASS);
  else c.template run<3>(ws PF_PASS);
  STAMP(0, 2);
  float* sc = (float*)smem;
  if (g == 0) {
    sc[wave * 16 + i] = c.rs[0];
    sc[1792 + wn * 96 + wm * 16 + i] = c.cs[0][0];
    if (wm + 4 < NB) sc[1792 + wn * 96 + (wm + 4) * 16 + i] = c.cs[1][0];
  }
  if (MODE != 2 && g == (i >> 2)) {
    const float s01 = (i & 1) ? c.sq[1] : c.sq[0], s23 = (i & 1) ? c.sq[3] : c.sq[2];
    sc[256 + wave * 16 + i] = (i & 2) ? s23 : s01;
  }
  LGKM_BAR();
  if (tid < 128) {
    if (MODE == 2) {
      sc[1536 + tid] = -bfr(sc[tid] * (1.0f / D_));
    } else {
      float mean_b, std_b;
      ln_stats(sc[tid], sc[256 + tid], mean_b, std_b);
      const float r = 1.0f / std_b;
      sc[1280 + tid] = r; sc[1536 + tid] = -mean_b * r;
      if (ct == 0) __hip_atomic_store((gu32*)(ws + WS_ST) + rb * 128 + tid, __float_as_uint(r * r), RLX);
    }
  }
  LGKM_BAR();
  if (MODE == 0) {
    const bool isq = ct < 4;
    const float scl = isq ? 0.22360679774997896f * 1.4426950408889634f : 1.0f;
    const int head = (ct & 3) * 8 + wn * 4 + g;
    f32x4 cv[5];
#pragma unroll
    for (int j = 0; j < 5; j++) cv[j] = *(const f32x4*)(sc + 1792 + wn * 96 + j * 16 + 4 * g);
#pragma unroll
    for (int a = 0; a < 2; a++) {
      const int blk = a ^ wn, row = wm * 32 + blk * 16 + i, odd = wm & 1;
      const float r = sc[1280 + row] * scl, m = sc[1536 + row] * scl;
      unsigned w[10];
#pragma unroll
      for (int j = 0; j < 5; j++) {
        w[2 * j] = pk2(c.acc[j][a][0] * r + m * cv[j][0], c.acc[j][a][1] * r + m * cv[j][1]);
        w[2 * j + 1] = pk2(c.acc[j][a][2] * r + m * cv[j][2], c.acc[j][a][3] * r + m * cv[j][3]);
      }
      const int rr = isq ? blk * 16 + i : 16 * (i >> 3) + 8 * blk + (i & 7);
      char* tb = isq ? ws + WS_Q + (head * 64 + rb * 4 + wm) * QT : ws + WS_KV + head * KVH + (rb * 2 + (wm >> 1)) * KVP + odd * KVT;
      ST16(tb + rr * 16, (u32x4{w[0], w[1], w[2], w[3]}));
      ST16(tb + 512 + rr * 16, (u32x4{w[4], w[5], w[6], w[7]}));
      ST8(tb + (isq ? 1024 + rr * 8 : 2 * KVT - odd * (KVT - 8) + rr * 16), (u32x2{w[8], w[9]}));
    }
  } else if (MODE == 1) {
    {
      const int odd = wm & 1, row = wm * 32 + 4 * g;
      const f32x4 r0 = *(const f32x4*)(sc + 1280 + row), r1 = *(const f32x4*)(sc + 1296 + row);
      const f32x4 m0 = *(const f32x4*)(sc + 1536 + row), m1 = *(const f32x4*)(sc + 1552 + row);
#pragma unroll
      for (int j = 0; j < 5; j++) {
        const int n = 16 * j + i, hl = (n * 52429) >> 20, d = n - hl * 20;
        const float cv = sc[1792 + wn * 96 + n];
        const f32x4 u = (wn ? c.acc[j][1] : c.acc[j][0]) * r0 + m0 * cv, v = (wn ? c.acc[j][0] : c.acc[j][1]) * r1 + m1 * cv;
        char* tb = smem + 8192 + wave * 5632 + hl * 1408 + g * VB + d * 16;
        *(u32x4*)tb = u32x4{pk2(u[0], u[1]), pk2(u[2], u[3]), pk2(v[0], v[1]), pk2(v[2], v[3])};
        if (j > 0 && d >= 18) { const unsigned k = (d == 18) ? 0x3f803f80u : 0u; *(u32x4*)(tb + 32) = u32x4{k, k, k, k}; }
      }
      asm volatile("s_waitcnt lgkmcnt(0)" ::: "memory");
      char* vb = ws + WS_KV + ((ct & 3) * 8 + wn * 4) * KVH + (rb * 2 + (wm >> 1)) * KVP + odd * KVT + 1024;
#pragma unroll
      for (int e = 0; e < 6; e++) {
        const int n = e * 64 + lane, hl = n / 88;
        if (n < 352) ST16(vb + hl * (KVH - 1408) + n * 16, *(const u32x4*)(smem + 8192 + wave * 5632 + n * 16));
      }
    }
  } else {
    f32x4 cv[6];
#pragma unroll
    for (int j = 0; j < 6; j++) cv[j] = *(const f32x4*)(sc + 1792 + wn * 96 + j * 16 + 4 * g);
#pragma unroll
    for (int a = 0; a < 2; a++) {
      const int sub = (wm & 1) * 2 + (a ^ wn);
      const float m = sc[1536 + wm * 32 + (a ^ wn) * 16 + i];
      unsigned w[6];
#pragma unroll
      for (int j = 0; j < 3; j++)
#pragma unroll
        for (int e = 0; e < 2; e++) {
          const unsigned gp = pk2(c.acc[j][a][2 * e] + m * cv[j][2 * e], c.acc[j][a][2 * e + 1] + m * cv[j][2 * e + 1]);
          const unsigned up = pk2(c.acc[j + 3][a][2 * e] + m * cv[j + 3][2 * e], c.acc[j + 3][a][2 * e + 1] + m * cv[j + 3][2 * e + 1]);
          w[2 * j + e] = pk2(bflo(gp) * bflo(up), bfhi(gp) * bfhi(up));
        }
      char* ab = ws + WS_A3 + (((rb * 2 + (wm >> 1)) * KS3 + 20 + ct * 3) * 4 + sub) * 1024;
      ST16(ab + wn * 8192 + lane * 16, (u32x4{w[0], w[1], w[2], w[3]}));
      ST8(ab + 4096 + wn * 512 + (g >> 1) * 256 + i * 16 + (g & 1) * 8, (u32x2{w[4], w[5]}));
    }
  }
}

__device__ __forceinline__ void retile_role(char* smem, const u16* __restrict__ wo, const u16* __restrict__ wd, char* __restrict__ ws,
                                            int idx, int tid, int wave) {
  const int lane = tid & 63, g = lane >> 4, q = (lane & 15) >> 2, p = lane & 3;
  const unsigned lds0 = (unsigned)(uintptr_t)smem;
  for (int k0 = idx; k0 < KS3; k0 += 64) {
    WAIT_BAR(0);
    for (int u = 0; u < 2; u++) {
      const int ks = k0 + 32 * u;
      if (ks < KS3) {
        const int orow = ks < 16 ? (2 * ks + (wave >> 2)) * 20 + (wave & 2) * 2 + (wave & 1) * 8 : (8 * (ks - 16) + wave) * 20 + 16;
        const char* src = (const char*)(ks < 20 ? wo + (size_t)orow * D_ : wd + (size_t)((ks - 20) * 32 + wave * 4) * D_) + lane * 16;
#pragma unroll
        for (int e = 0; e < 5; e++) glds16(src + e * 1024, lds0 + u * 40960 + wave * 5120 + e * 1024);
      }
    }
    WAIT_BAR(0);
    for (int u = 0; u < 2; u++) {
      const int ks = k0 + 32 * u;
      if (ks < KS3) {
        const bool nat = ks < 20 || ks % 3 == 0;
        const int row = nat ? 8 * g + q : 4 * g + q, sec = (nat ? 4 : 16) * 1280;
        lds_cptr b = (lds_cptr)smem + u * 40960 + row * 1280 + wave * 160;
#pragma unroll
        for (int t = 0; t < 5; t++)
          ST16(ws + WS_W3 + ((wave * KS3 + ks) * 5 + t) * 1024 + lane * 16, __builtin_bit_cast(u32x4, ldstr2(b + (t < 4 ? (t >> 1) * 64 + p * 16 + (t & 1) * 8 : 128 + p * 8), sec)));
      }
    }
  }
}

constexpr int AT_BUF = 4 * KVP;
constexpr int K2_LDS = 81920;

#define TOFF(t) (((t) >> 1) * KVP + ((t) & 1) * KVT)
struct KFrag { bf16x8 k1; s16x4 k2; };

__device__ __forceinline__ f32x16 qk_tile(const KFrag& k, bf16x8 q1, s16x4 q2) {
  f32x16 z = {};
  f32x16 s = MFMA32(k.k1, q1, z);
  return MFMA32X8(k.k2, q2, s);
}
__device__ __forceinline__ bf16x8 exp_pack(const f32x16& s, int m) {
  u32x4 w;
#pragma unroll
  for (int i = 0; i < 4; i++) w[i] = pk2(__builtin_amdgcn_exp2f(s[8 * m + 2 * i]), __builtin_amdgcn_exp2f(s[8 * m + 2 * i + 1]));
  return __builtin_bit_cast(bf16x8, w);
}
__device__ __forceinline__ float max16(const f32x16& s) {
  float a = fmaxf(s[0], s[1]);
#pragma unroll
  for (int i = 2; i < 16; i += 2) a = fmaxf(fmaxf(a, s[i]), s[i + 1]);
  return a;
}

#define AT_GROUP(MF, ACC, A, B, P, PJ, SJ) do { unsigned pa_, pb_; SBAR();                                                        \
    asm volatile(MF "\n\tv_cvt_pk_bf16_f32 %1, %3, %4\n\tv_cvt_pk_bf16_f32 %2, %5, %6\n\tv_exp_f32 %3, %9\n\tv_exp_f32 %4, %10\n\t"    \
                 "v_exp_f32 %5, %11\n\tv_exp_f32 %6, %12"                                                                          \
                 : ACC, "=&v"(pa_), "=&v"(pb_), "+v"(e[0]), "+v"(e[1]), "+v"(e[2]), "+v"(e[3])                                     \
                 : "v"(A), "v"(B), "v"(sa[SJ]), "v"(sa[SJ + 1]), "v"(sa[SJ + 2]), "v"(sa[SJ + 3]));                                \
    P[PJ] = pa_; P[PJ + 1] = pb_; SBAR(); } while (0)

__device__ __forceinline__ void attn_role(char* smem, char* __restrict__ ws, unsigned tag, int head, int qb, int tid, int wave PF_ARG) {
  const int lane = tid & 63, r = lane & 31, hh = lane >> 5;
  const unsigned lds0 = (unsigned)(uintptr_t)smem;
  const char* kvsrc = ws + WS_KV + (size_t)head * KVH + wave * 1024 + lane * 16;
  auto dma = [&](int c, int buf) {
    const char* src = kvsrc + (size_t)c * AT_BUF;
    const unsigned dst = lds0 + buf + wave * 1024;
    glds16d(src, dst);
    glds16d(src + 8192, dst + 8192);
    if (wave < 5) glds16d(src + 16384, dst + 16384);
  };
  if (wave == 0) poll(ws, FQ + (head >> 3) * 64 + min(lane, 47), tag);
  LGKM_BAR();
  STAMP(1, 0);
  dma(0, 0);
  const int qo = (int)WS_Q + (head * 64 + qb * 8 + wave) * QT;
  const bf16x8 q1 = LD16(lane * 16, qo);
  u32x2 q2u = LD8(1024 + r * 8, qo);
  if (hh) q2u = u32x2{0u, 0u};
  const int k1o = lane * 16, k2o = hh ? 1024 + 320 : 2 * KVT + r * 16, vo = 1024 + hh * VB + min(r, 21) * 16;
  lds_cptr lb = (lds_cptr)smem;
  LAS int* flag = (LAS int*)(lb + 3 * AT_BUF + 512);
  auto ldk = [&](int t) { KFrag k; k.k1 = lds128(lb + TOFF(t) + k1o); k.k2 = lds64(lb + (t >> 1) * KVP + (t & 1) * 8 + k2o); return k; };
  auto set_ref = [&](float m) {
    const float m0 = bfr(m), m1 = bfr(m - m0), m2 = bfr(m - m0 - m1);
    if (hh) q2u = u32x2{pk2(-m0, -m1), pk2(-m2, 0.f)};
  };
  WAIT_BAR(0);
  dma(1, AT_BUF);
  {
    KFrag k = ldk(0);
    set_ref(xhalf_max(max16(qk_tile(k, q1, __builtin_bit_cast(s16x4, q2u)))));
  }
  f32x16 o;
  STAMP(1, 1);
  for (int attempt = 0;; attempt++) {
    const s16x4 q2 = __builtin_bit_cast(s16x4, q2u);
    o = f32x16{};
    f32x16 sa;
    { KFrag k = ldk(0); sa = qk_tile(k, q1, q2); }
    bf16x8 k1 = lds128(lb + KVT + k1o), v1 = {}, v0;
    s16x8 k2p = __builtin_bit_cast(s16x8, lds128(lb + k2o));
    v0 = lds128(lb + vo);
    u32x4 p0 = {0u, 0u, 0u, 0u}, p1 = {0u, 0u, 0u, 0u};
    float e[4] = {0.f, 0.f, 0.f, 0.f};
    int cur = 0, nxt = AT_BUF;
    asm volatile("s_nop 15\n\ts_nop 7" : "+v"(sa), "+v"(o), "+v"(p1), "+v"(v1));
    for (int c = 0; c < 8; c++) {
      lds_cptr ck1 = lb + cur + k1o, ck2 = lb + cur + k2o, cv = lb + cur + vo;
      lds_cptr nk1 = lb + nxt + k1o, nk2 = lb + nxt + k2o, nv = lb + nxt + vo;
      const int nn = (nxt == 2 * AT_BUF) ? 0 : nxt + AT_BUF;
#pragma unroll
      for (int t = 0; t < 8; t++) {
        XL(8) if (t == 6) asm volatile("s_waitcnt vmcnt(0)\n\ts_barrier" ::: "memory");
        const int pc = (t + 2) & 7;
        const bool ld = pc < 3 && (pc < 2 ? c < 6 : (c > 0 && c < 7 && wave < 5));
        const char* src = kvsrc + (size_t)(t == 0 ? c + 1 : c + 2) * AT_BUF + pc * 8192;
        const unsigned dst = lds0 + (t == 0 ? nxt : nn) + wave * 1024 + pc * 8192;
        XL(8) if (ld && wave < 4) glds16d(src, dst);
        f32x16 sb;
        const s16x4 k2 = (t & 1) ? __builtin_shufflevector(k2p, k2p, 0, 1, 2, 3) : __builtin_shufflevector(k2p, k2p, 4, 5, 6, 7);
        AT_GROUP("v_mfma_f32_32x32x16_bf16 %0, %7, %8, 0", "=&v"(sb), k1, q1, p1, 2, 0);
        XL(1) k1 = lds128((t < 6 ? ck1 : nk1) + TOFF((t + 2) & 7));
        AT_GROUP("v_mfma_f32_32x32x16_bf16 %0, %7, %8, %0", "+v"(o), v1, p1, p0, 0, 4);
        XL(2) v1 = lds128(cv + TOFF(t) + 2 * VB);
        XL(8) if (ld && wave >= 4) glds16d(src, dst);
        AT_GROUP("v_mfma_f32_32x32x8_bf16 %0, %7, %8, %0", "+v"(sb), k2, q2, p0, 2, 8);
        XL(4) if (!(t & 1)) k2p = __builtin_bit_cast(s16x8, lds128((t < 6 ? ck2 : nk2) + TOFF((t + 2) & 7)));
        AT_GROUP("v_mfma_f32_32x32x16_bf16 %0, %7, %8, %0", "+v"(o), v0, p0, p1, 0, 12);
        XL(2) v0 = lds128((t < 7 ? cv : nv) + TOFF((t + 1) & 7));
        sa = sb;
      }
      cur = nxt; nxt = nn;
    }
    asm volatile("s_nop 7" : "+v"(o), "+v"(e[3]));
    p1[2] = pk2(e[0], e[1]); p1[3] = pk2(e[2], e[3]);
    o = MFMA32(v1, __builtin_bit_cast(bf16x8, p1), o);
    if (attempt == 1) break;
    bool ok = true;
#pragma unroll
    for (int i = 0; i < 12; i++) ok = ok && (fabsf(o[i]) < 1e30f || (hh && i > 8));
    LGKM_BAR();
    if (tid == 0) *flag = 0;
    LGKM_BAR();
    if (!ok) *flag = 1;
    LGKM_BAR();
    if (*flag == 0) break;
    if (hh) q2u = u32x2{0u, 0u};
    const s16x4 qz = __builtin_bit_cast(s16x4, q2u);
    float mx = -INFINITY;
    for (int c = 0; c < 8; c++) {
      LGKM_BAR();
      dma(c, 0);
      WAIT_BAR(0);
      for (int t = 0; t < 8; t++) { KFrag k = ldk(t); mx = fmaxf(mx, max16(qk_tile(k, q1, qz))); }
    }
    set_ref(xhalf_max(mx));
    LGKM_BAR();
    dma(0, 0); dma(1, AT_BUF);
    WAIT_BAR(0);
  }
  STAMP(1, 2);
  auto sw = __builtin_amdgcn_permlane32_swap(__float_as_uint(o[8]), __float_as_uint(o[8]), false, false);
  const float inv = 1.0f / __uint_as_float(sw[1]);
  const int s = qb * 256 + wave * 32 + r;
  char* ab = ws + WS_A3 + ((s >> 6) * KS3 * 4 + ((s >> 4) & 3)) * 1024;
  ST16(ab + (head >> 1) * 4096 + ((head & 1) * 2 + hh) * 256 + (s & 15) * 16,
       (u32x4{pk2(o[0] * inv, o[1] * inv), pk2(o[2] * inv, o[3] * inv), pk2(o[4] * inv, o[5] * inv), pk2(o[6] * inv, o[7] * inv)}));
  if (!hh) ST8(ab + (16 + (head >> 3)) * 4096 + (head & 7) * 128 + (s & 15) * 8, (u32x2{pk2(o[8] * inv, o[9] * inv), pk2(o[10] * inv, o[11] * inv)}));
  publish(ws, FO + qb * 32 + head, tag, tid);
}

__device__ __forceinline__ void down_role(char* smem, const u16* __restrict__ x, char* __restrict__ ws, u16* __restrict__ out, unsigned tag, int rb, int nb, int tid, int wave PF_ARG) {
  const int lane = tid & 63;
  if (wave == 0) poll(ws, lane < 18 ? FG + (rb >> 1) * 32 + lane : lane < 32 ? FQ + (rb >> 1) : FR - 32 + lane, tag);
  LGKM_BAR();
  STAMP(1, 0);
  int ap = (int)WS_A3 + (rb * KS3 + 20 + wave) * 4096, wp = (int)WS_W3 + (nb * KS3 + 20 + wave) * 5120;
  bf16x8 a[4], w[5];
#pragma unroll
  for (int t = 0; t < 5; t++) w[t] = LD16(lane * 16 + (t & 3) * 1024, wp + (t >> 2) * 4096);
#pragma unroll
  for (int i = 0; i < 4; i++) a[i] = LD16(lane * 16 + i * 1024, ap);
  f32x4 acc[20] = {};
  const int n = wave < 6 ? 7 : 6;
  for (int it = 1; it <= n; it++) {
    if (it < n) { ap += 8 * 4096; wp += 8 * 5120; }
#pragma unroll
    for (int i = 0; i < 4; i++) {
#pragma unroll
      for (int t = 0; t < 5; t++) acc[t * 4 + i] = MFMA16(w[t], a[i], acc[t * 4 + i]);
      SBAR();
      a[i] = LD16(lane * 16 + i * 1024, ap);
    }
#pragma unroll
    for (int t = 0; t < 5; t++) w[t] = LD16(lane * 16 + (t & 3) * 1024, wp + (t >> 2) * 4096);
  }
  STAMP(1, 2);
  __builtin_amdgcn_s_setprio(X_PB3);
  f32x4* red = (f32x4*)smem;
  const int rbk = wave & 3, kg = wave >> 2, g = lane >> 4, row = rb * 64 + rbk * 16 + (lane & 15);
  const size_t xo = (size_t)row * D_ + nb * 80;
  const u32x4 xv = *(const u32x4*)(x + xo + kg * 32 + g * 8);
  const u32x2 xw = *(const u32x2*)(x + xo + 64 + g * 4);
  const float r2 = __uint_as_float(__hip_atomic_load((gu32*)(ws + WS_ST) + row, RLX));
  f32x4 fin[3];
#pragma unroll
  for (int rd = 0; rd < 3; rd++) {
    if (rd) LGKM_BAR();
#pragma unroll
    for (int u = 0; u < (rd < 2 ? 8 : 4); u++) red[(wave * 8 + u) * 64 + lane] = acc[(rd < 2 ? 2 * (u >> 2) + rd : 4) * 4 + (u & 3)];
    LGKM_BAR();
    f32x4 v = red[(rd < 2 ? wave : rbk) * 64 + lane];
#pragma unroll
    for (int k = 1; k < 8; k++) v += red[(k * 8 + (rd < 2 ? wave : rbk)) * 64 + lane];
    const unsigned x0 = rd < 2 ? xv[2 * rd] : xw[0], x1 = rd < 2 ? xv[2 * rd + 1] : xw[1];
    fin[rd] = v * r2 + f32x4{bflo(x0), bfhi(x0), bflo(x1), bfhi(x1)};
    PIN(fin[rd]);
  }
  LGKM_BAR();
  STAMP(1, 4);
  const char* wo = ws + WS_W3 + ((size_t)nb * KS3 + kg * 10) * 5120 + lane * 16;
  const unsigned lds0 = (unsigned)(uintptr_t)smem;
#pragma unroll
  for (int e = 0; e < 10; e++) glds16d(wo + (10 + rbk * 10 + e) * 1024, lds0 + (wave * 10 + e) * 1024);
  bf16x8 wr[10];
#pragma unroll
  for (int e = 0; e < 10; e++) wr[e] = LD16(lane * 16 + (e & 3) * 1024, (int)WS_W3 + (nb * KS3 + kg * 10) * 5120 + (e >> 2) * 4096);
  f32x4 c[5];
  c[0] = kg ? f32x4{} : fin[0]; c[1] = kg ? f32x4{} : fin[1]; c[2] = kg ? fin[0] : f32x4{}; c[3] = kg ? fin[1] : f32x4{}; c[4] = kg ? f32x4{} : fin[2];
#pragma unroll
  for (int e = 0; e < 10; e++) PIN(wr[e]);
  asm volatile("" : "+v"(c[0]), "+v"(c[1]), "+v"(c[2]), "+v"(c[3]), "+v"(c[4]));
  STAMP(1, 5);
  if (wave == 0) poll(ws, FO + (rb >> 2) * 32 + (lane & 31), tag);
  WAIT_BAR(0);
  STAMP(1, 6);
  const int ob = (int)WS_A3 + ((rb * KS3 + kg * 10) * 4 + rbk) * 1024, o2 = kg ? g * 256 + (lane & 15) * 8 : lane * 16, o3 = o2 + (kg ? 128 : 8);
  bf16x8 of[10];
#pragma unroll
  for (int k = 0; k < 10; k++) {
    if (k < 6) of[k] = LD16(lane * 16, ob + k * 4096);
    else {
      const u32x2 lo = LD8(o2, ob + k * 4096), hi = LD8(o3, ob + k * 4096);
      of[k] = __builtin_bit_cast(bf16x8, u32x4{lo[0], lo[1], hi[0], hi[1]});
    }
  }
  SBAR();
  lds_cptr lw = (lds_cptr)smem + kg * 40960 + lane * 16;
#pragma unroll
  for (int k = 0; k < 10; k++) {
#pragma unroll
    for (int t = 0; t < 5; t++) c[t] = MFMA16(wr[(k & 1) * 5 + t], of[k], c[t]);
    asm volatile("" : "+v"(c[0]), "+v"(c[1]), "+v"(c[2]), "+v"(c[3]), "+v"(c[4]) :: "memory");
    if (k < 8)
#pragma unroll
      for (int t = 0; t < 5; t++) wr[(k & 1) * 5 + t] = lds128(lw + (k * 5 + t) * 1024);
    SBAR();
  }
  LGKM_BAR();
  red[(wave * 3) * 64 + lane] = kg ? c[0] : c[2];
  red[(wave * 3 + 1) * 64 + lane] = kg ? c[1] : c[3];
  if (kg) red[(wave * 3 + 2) * 64 + lane] = c[4];
  LGKM_BAR();
  const f32x4* pr = red + ((wave ^ 4) * 3) * 64 + lane;
  const f32x4 v0 = (kg ? c[2] : c[0]) + pr[0], v1 = (kg ? c[3] : c[1]) + pr[64];
  *(u32x4*)(out + xo + kg * 32 + g * 8) = u32x4{pk2(v0[0], v0[1]), pk2(v0[2], v0[3]), pk2(v1[0], v1[1]), pk2(v1[2], v1[3])};
  if (!kg) { const f32x4 v2 = c[4] + pr[128]; *(u32x2*)(out + xo + 64 + g * 4) = u32x2{pk2(v2[0], v2[1]), pk2(v2[2], v2[3])}; }
}

#define X_PRI1 2
#define X_PRI2 0
__global__ __launch_bounds__(512, 4) void fused_block(const u16* __restrict__ x, const u16* __restrict__ wqkv, const u16* __restrict__ wg,
                                                      const u16* __restrict__ wu, const u16* __restrict__ wo, const u16* __restrict__ wd,
                                                      char* __restrict__ ws, u16* __restrict__ out) {
  __shared__ __attribute__((aligned(16))) char smem[K2_LDS];
  const int tid = threadIdx.x;
  const int wave = __builtin_amdgcn_readfirstlane(tid >> 6);
  const int b = blockIdx.x, xcd = b & 7, o = (b >> 3) & 31, cg = xcd >> 1, t = o >> 3, rb = (xcd & 1) * 8 + (o & 7);
  PF_DECL; STAMP(0, 0);
  int slot;
  if (b >= 256) {
    const int u = min(tid - 320, 159);
    const char* pf = u < 0 ? (const char*)(x + (rb * 128 + t * 32) * D_) + tid * 128 : (const char*)(wqkv + (o + (u >> 3) * 32) * N1_ + cg * 480) + (u & 7) * 128;
    const unsigned dst = (unsigned)(uintptr_t)smem + 3 * STG + wave * 256;
    asm volatile("s_mov_b32 m0, %1\n\ts_nop 0\n\tglobal_load_lds_dword %0, off" :: "v"(pf), "s"(dst) : "memory", "m0");
    if (wave < 5) {
      pf = (const char*)((tid & 2 ? wu : wg) + ((o & 7) + (tid >> 2) * 8) * F_ + ((cg >> 1) * 9 + (cg & 1) * 5 + t) * 96) + (tid & 1) * 188;
      asm volatile("s_mov_b32 m0, %1\n\ts_nop 0\n\tglobal_load_lds_dword %0, off" :: "v"(pf), "s"(dst) : "memory", "m0");
    }
  }
  if (b < 256) __builtin_amdgcn_s_setprio(X_PRI1); else {
    __builtin_amdgcn_s_setprio(X_PB1);
    for (int i = 0; i < X_BS; i++) __builtin_amdgcn_s_sleep(32);
  }
  if (b < 256 && t < 3) {
    const int ct = cg * 3 + t;
    if (ct < 8) k1_tile<0>(smem, x, wqkv, wqkv, ws, rb, ct, tid, wave PF_PASS);
    else k1_tile<1>(smem, x, wqkv, wqkv, ws, rb, ct, tid, wave PF_PASS);
    slot = FQ + (ct & 3) * 64 + (ct >> 2) * 16 + rb;
  } else if (b >= 256 || !(cg & 1)) {
    const int ct = (cg >> 1) * 9 + (b < 256 ? 4 : (cg & 1) * 5 + t);
    k1_tile<2>(smem, x, wg, wu, ws, rb, ct, tid, wave PF_PASS);
    slot = FG + rb * 32 + ct;
  } else {
    retile_role(smem, wo, wd, ws, (cg >> 1) * 16 + rb, tid, wave);
    slot = FR + (cg >> 1) * 16 + rb;
  }
  STAMP(0, 4);
  const unsigned tag = __builtin_amdgcn_readfirstlane(__hip_atomic_load((gu32*)(ws + WS_FLG), RLX)) + 1u;
  publish(ws, slot, tag, tid);
  STAMP(0, 3);
  if (b < 256) {
    __builtin_amdgcn_s_setprio(X_PRI2);
    attn_role(smem, ws, tag, xcd * 4 + (o >> 3), o & 7, tid, wave PF_PASS);
  } else {
    __builtin_amdgcn_s_setprio(X_PB2);
    down_role(smem, x, ws, out, tag, (xcd & 3) * 8 + (o & 7), (xcd >> 2) * 4 + (o >> 3), tid, wave PF_PASS);
    if (b == 256) {
      poll(ws, FG + (tid >> 5) * 32 + min(tid & 31, 17), tag);
      publish(ws, 0, tag, tid);
    }
  }
  STAMP(1, 3);
}

extern "C" void kernel_launch(void* const* d_in, const int* in_sizes, int n_in, void* d_out, int out_size, void* d_ws, size_t ws_size,
                              hipStream_t stream) {
  fused_block<<<512, 512, 0, stream>>>((const u16*)d_in[0], (const u16*)d_in[1], (const u16*)d_in[4], (const u16*)d_in[5], (const u16*)d_in[2],
                                       (const u16*)d_in[6], (char*)d_ws, (u16*)d_out);
}
```

```cpp
#include <hip/hip_runtime.h>
#include <hip/hip_bf16.h>

typedef unsigned short u16;
typedef __attribute__((ext_vector_type(8))) __bf16 bf16x8;
typedef __attribute__((ext_vector_type(2))) __bf16 bf16x2;
typedef __attribute__((ext_vector_type(4))) short s16x4;
typedef __attribute__((ext_vector_type(8))) short s16x8;
typedef __attribute__((ext_vector_type(16))) float f32x16;
typedef __attribute__((ext_vector_type(4))) float f32x4;
typedef __attribute__((ext_vector_type(2))) float f32x2;
typedef __attribute__((ext_vector_type(4))) unsigned u32x4;
typedef __attribute__((ext_vector_type(2))) unsigned u32x2;
#define LAS __attribute__((address_space(3)))
typedef LAS const char* lds_cptr;
typedef __attribute__((address_space(1))) unsigned gu32;

constexpr int S_ = 2048, D_ = 640, F_ = 1728, N1_ = 1920;
constexpr int QT = 1280;
constexpr int VB = 352;
constexpr int KVT = 1024 + 4 * VB;
constexpr int KVP = 2 * KVT + 512;
constexpr int KVH = 32 * KVP;
constexpr int KS3 = 74;
constexpr size_t WS_ST = 0;
constexpr size_t WS_Q = WS_ST + 16384;
constexpr size_t WS_KV = WS_Q + (size_t)32 * 64 * QT;
constexpr size_t WS_A3 = WS_KV + (size_t)32 * KVH;
constexpr size_t WS_W3 = WS_A3 + (size_t)32 * KS3 * 4096;
constexpr size_t WS_FLG = WS_W3 + (size_t)8 * KS3 * 5120;
constexpr int FQ = 64, FG = 512, FR = 1024, FO = 1152;

#define MFMA32(a, b, c) __builtin_amdgcn_mfma_f32_32x32x16_bf16(a, b, c, 0, 0, 0)
#define MFMA32X8(a, b, c) __builtin_amdgcn_mfma_f32_32x32x8bf16_1k(a, b, c, 0, 0, 0)
#define MFMA16(a, b, c) __builtin_amdgcn_mfma_f32_16x16x32_bf16(a, b, c, 0, 0, 0)
#define SGB(m, n) __builtin_amdgcn_sched_group_barrier(m, n, 0)
#define WSR __builtin_amdgcn_make_buffer_rsrc(ws, 0, (int)WS_FLG, 0x00020000)
#define ST16(p, v) __builtin_amdgcn_raw_buffer_store_b128(v, WSR, (int)((p) - ws), 0, 16)
#define ST8(p, v) __builtin_amdgcn_raw_buffer_store_b64(v, WSR, (int)((p) - ws), 0, 16)
#define LD16(vo, so) __builtin_bit_cast(bf16x8, __builtin_amdgcn_raw_buffer_load_b128(WSR, vo, so, 16))
#define LD8(vo, so) __builtin_amdgcn_raw_buffer_load_b64(WSR, vo, so, 16)
#define RLX __ATOMIC_RELAXED, __HIP_MEMORY_SCOPE_AGENT
#define SBAR() __builtin_amdgcn_sched_barrier(0)
#define PIN(x) asm volatile("" : "+v"(x))
#define WAIT_BAR(N) asm volatile("s_waitcnt vmcnt(" #N ") lgkmcnt(0)\n\ts_barrier" ::: "memory")
#define LGKM_BAR() asm volatile("s_waitcnt lgkmcnt(0)\n\ts_barrier" ::: "memory")

#define X_PH 2
#define X_POLL 4
#define X_STG 4
#define X_PB1 1
#define X_PB2 0
#define X_PB3 0
#define X_BS 0
#define X_PF 2
#define X_PC 3
#define X_LM 0
#define XL(b) if (!(X_LM & b))
#define PF_DECL
#define PF_ARG
#define PF_PASS
#define STAMP(k, i)
__device__ __forceinline__ void glds16(const void* g, unsigned lds_base) {
  asm volatile("s_mov_b32 m0, %1\n\ts_nop 0\n\tglobal_load_lds_dwordx4 %0, off" :: "v"(g), "s"(lds_base) : "memory", "m0");
}
__device__ __forceinline__ void glds16s(const void* base, unsigned off, unsigned lds_base) {
  asm volatile("s_mov_b32 m0, %2\n\ts_nop 0\n\tglobal_load_lds_dwordx4 %0, %1" :: "v"(off), "s"(base), "s"(lds_base) : "memory", "m0");
}
__device__ __forceinline__ void glds16d(const void* g, unsigned lds_base) {
  asm volatile("s_mov_b32 m0, %1\n\ts_nop 0\n\tglobal_load_lds_dwordx4 %0, off sc1" :: "v"(g), "s"(lds_base) : "memory", "m0");
}
__device__ __forceinline__ void publish(char* ws, int slot, unsigned tag, int tid) {
  WAIT_BAR(0);
  if (tid == 0) __hip_atomic_store((gu32*)(ws + WS_FLG) + slot, tag, RLX);
}
__device__ __forceinline__ void poll(char* ws, int slot, unsigned tag) {
  for (int i = 0; i < (1 << 18); i++) {
    if (__all(__hip_atomic_load((gu32*)(ws + WS_FLG) + slot, RLX) == tag)) break;
    __builtin_amdgcn_s_sleep(X_POLL);
  }
}
__device__ __forceinline__ unsigned pk2(float lo, float hi) {
  f32x2 v = {lo, hi};
  return __builtin_bit_cast(unsigned, __builtin_convertvector(v, bf16x2));
}
__device__ __forceinline__ float bfr(float v) { return (float)(__bf16)v; }
__device__ __forceinline__ float bflo(unsigned u) { return __uint_as_float(u << 16); }
__device__ __forceinline__ float bfhi(unsigned u) { return __uint_as_float(u & 0xffff0000u); }
__device__ __forceinline__ bf16x8 lds128(lds_cptr p) { return *(const LAS bf16x8*)p; }
__device__ __forceinline__ s16x4 lds64(lds_cptr p) { return *(const LAS s16x4*)p; }
__device__ __forceinline__ bf16x8 ldstr2(lds_cptr p, int second) {
  s16x4 a = __builtin_amdgcn_ds_read_tr16_b64_v4i16((LAS s16x4*)p);
  s16x4 b = __builtin_amdgcn_ds_read_tr16_b64_v4i16((LAS s16x4*)(p + second));
  return __builtin_bit_cast(bf16x8, __builtin_shufflevector(a, b, 0, 1, 2, 3, 4, 5, 6, 7));
}
__device__ __forceinline__ float xhalf_max(float v) {
  auto r = __builtin_amdgcn_permlane32_swap(__float_as_uint(v), __float_as_uint(v), false, false);
  return fmaxf(__uint_as_float(r[0]), __uint_as_float(r[1]));
}
__device__ __forceinline__ void ln_stats(float sum, float ssq, float& mean_b, float& std_b) {
  float mean = sum * (1.0f / D_);
  float var = fmaxf(ssq * (1.0f / D_) - mean * mean, 0.0f);
  mean_b = bfr(mean);
  std_b = bfr(sqrtf(bfr(bfr(var) + 1e-5f)));
}

constexpr int STG = 12288, X0 = 4 * STG;
#define WAIT_BARN(N) asm volatile("s_waitcnt vmcnt(%0) lgkmcnt(0)\n\ts_barrier" :: "n"(N) : "memory")

template <int MODE>
struct K1Core {
  static constexpr int NB = MODE == 2 ? 6 : 5;
  f32x4 acc[NB][2], cs[2], rs, sq;
  bf16x8 sel;
  const u16 *xs, *wpa, *wpb;
  unsigned xl, wla, wlb;
  int wstep, xo, wo[5], wave;
  unsigned lds0;
  lds_cptr lb;
  static constexpr int ROW = MODE == 2 ? 192 : 320;
  __device__ __forceinline__ void issue_x(int h) {
    const unsigned dst = lds0 + X0 + (h & 2) * 8192 + wave * 2048;
    glds16s(xs + h * 32, xl, dst); glds16s(xs + h * 32 + 8 * D_, xl ^ 64, dst + 1024);
  }
  template <int PAR, int WM> __device__ __forceinline__ void issue_w(int h) {
    const unsigned dst = lds0 + (h & 3) * STG;
    glds16s(wpa + h * wstep, wla, dst + wave * 1024);
    if (MODE == 2 ? (WM >> 1) == PAR : WM == 2 * PAR) glds16s(wpb + h * wstep, wlb, dst + (8 + (wave & 3)) * 1024);
  }
  template <int PAR, int WM> __device__ __forceinline__ void step(int h, bool more) {
    int so = (h & 3) * STG, sx = (h & 2) * 8192;
    asm volatile("" : "+s"(so), "+s"(sx));
    lds_cptr st = lb + so;
    bf16x8 xf[2], wf[NB];
    xf[0] = lds128(lb + sx + (xo ^ (PAR * 64))); xf[1] = lds128(lb + sx + (xo ^ (PAR * 64) ^ 2048));
#pragma unroll
    for (int j = 0; j < NB; j++) wf[j] = ldstr2(st + (MODE == 2 ? wo[j % 3] + (j / 3) * 6144 : wo[j]), 4 * ROW);
    SBAR();
    if (more && (WM < X_STG)) { if (!PAR) issue_x(h + 2); issue_w<PAR ^ 1, WM>(h + 3); }
    SBAR();
    rs = MFMA16(sel, xf[0], rs);
    if (MODE != 2) sq = MFMA16(xf[0], xf[0], sq);
#pragma unroll
    for (int j = 0; j < NB; j++) {
#pragma unroll
      for (int a = 0; a < 2; a++) acc[j][a] = MODE == 1 ? MFMA16(xf[a], wf[j], acc[j][a]) : MFMA16(wf[j], xf[a], acc[j][a]);
      if ((j & 3) == WM) cs[j >> 2] = MFMA16(sel, wf[j], cs[j >> 2]);
    }
    SBAR();
#pragma unroll
    for (int j = 0; j < NB; j++) asm volatile("" : "+v"(acc[j][0]), "+v"(acc[j][1]));
    asm volatile("" : "+v"(cs[0]), "+v"(rs));
    if (WM + 4 < NB) PIN(cs[1]);
    if (MODE != 2) PIN(sq);
    if (more && !(WM < X_STG)) { if (!PAR) issue_x(h + 2); issue_w<PAR ^ 1, WM>(h + 3); }
  }
  template <int WM> __device__ __forceinline__ void run(char* ws PF_ARG) {
    constexpr int W2 = (MODE == 2 || !(WM & 1)) ? 3 : 2;
    issue_w<0, WM>(0); issue_x(0); issue_w<1, WM>(1); issue_w<0, WM>(2);
#pragma unroll
    for (int j = 0; j < NB; j++) acc[j][0] = acc[j][1] = f32x4{};
    cs[0] = cs[1] = rs = sq = f32x4{};
    for (int h = 0; h < 18; h += 2) {
      WAIT_BARN(W2); step<0, WM>(h, true);
      WAIT_BARN(W2 + 2); step<1, WM>(h + 1, h < 16);
    }
    WAIT_BAR(0); step<0, WM>(18, false); step<1, WM>(19, false);
  }
};

template <int MODE>
__device__ __forceinline__ void k1_tile(char* smem, const u16* __restrict__ x, const u16* __restrict__ w0, const u16* __restrict__ w1,
                                        char* __restrict__ ws, int rb, int ct, int tid, int wave PF_ARG) {
  constexpr int NB = K1Core<MODE>::NB, LD = MODE == 2 ? F_ : N1_;
  const int lane = tid & 63, i = lane & 15, g = lane >> 4, wm = wave >> 1, wn = wave & 1;
  K1Core<MODE> c;
  c.wave = wave; c.lds0 = (unsigned)(uintptr_t)smem; c.lb = (lds_cptr)smem; c.wstep = 32 * LD;
  c.xs = x + (rb * 128 + wave * 16) * D_;
  c.xl = (lane >> 3) * D_ * 2 + (((lane & 7) ^ g) << 4);
  {
    constexpr int CPR = K1Core<MODE>::ROW / 16;
    const int pa = MODE == 2 && wave >= 6 ? wave - 6 : wave, pb = MODE == 2 ? 2 + (wave & 3) : 8 + (wave & 1);
    const int ca = pa * 64 + lane, ra = ca / CPR, cb = pb * 64 + lane, rc = cb / CPR;
    c.wla = ra * LD * 2 + (((ca - ra * CPR) ^ ((ra >> 2) & 2)) << 4);
    c.wlb = rc * LD * 2 + (((cb - rc * CPR) ^ ((rc >> 2) & 2)) << 4);
    c.wpa = (wave >= 6 ? w1 : w0) + ct * (CPR * 8);
    c.wpb = w1 + ct * (CPR * 8);
  }
  c.xo = X0 + wave * 2048 + i * 128 + ((g ^ (i >> 1)) << 4);
  {
    const int q = i >> 2, p = lane & 3;
#pragma unroll
    for (int j = 0; j < 5; j++) {
      const int Q = MODE == 0 ? 5 * p + j + 20 * wn : MODE == 1 ? 4 * j + p + 20 * wn : 4 * (wn ? (j == 2 ? 3 : 4 + j) : j) + p;
      c.wo[j] = (8 * g + q) * K1Core<MODE>::ROW + ((Q ^ (4 * (g & 1))) << 3);
    }
    const unsigned v = i == 0 ? 0x3f803f80u : 0u;
    c.sel = __builtin_bit_cast(bf16x8, u32x4{v, v, v, v});
  }
  if (wm == 0) c.template run<0>(ws PF_PASS);
  else if (wm == 1) c.template run<1>(ws PF_PASS);
  else if (wm == 2) c.template run<2>(ws PF_PASS);
  else c.template run<3>(ws PF_PASS);
  STAMP(0, 2);
  float* sc = (float*)smem;
  if (g == 0) {
    sc[wave * 16 + i] = c.rs[0];
    sc[1792 + wn * 96 + wm * 16 + i] = c.cs[0][0];
    if (wm + 4 < NB) sc[1792 + wn * 96 + (wm + 4) * 16 + i] = c.cs[1][0];
  }
  if (MODE != 2 && g == (i >> 2)) {
    const float s01 = (i & 1) ? c.sq[1] : c.sq[0], s23 = (i & 1) ? c.sq[3] : c.sq[2];
    sc[256 + wave * 16 + i] = (i & 2) ? s23 : s01;
  }
  LGKM_BAR();
  if (tid < 128) {
    if (MODE == 2) {
      sc[1536 + tid] = -bfr(sc[tid] * (1.0f / D_));
    } else {
      float mean_b, std_b;
      ln_stats(sc[tid], sc[256 + tid], mean_b, std_b);
      const float r = 1.0f / std_b;
      sc[1280 + tid] = r; sc[1536 + tid] = -mean_b * r;
      if (ct == 0) __hip_atomic_store((gu32*)(ws + WS_ST) + rb * 128 + tid, __float_as_uint(r * r), RLX);
    }
  }
  LGKM_BAR();
  if (MODE == 0) {
    const bool isq = ct < 4;
    const float scl = isq ? 0.22360679774997896f * 1.4426950408889634f : 1.0f;
    const int head = (ct & 3) * 8 + wn * 4 + g;
    f32x4 cv[5];
#pragma unroll
    for (int j = 0; j < 5; j++) cv[j] = *(const f32x4*)(sc + 1792 + wn * 96 + j * 16 + 4 * g);
#pragma unroll
    for (int a = 0; a < 2; a++) {
      const int blk = a ^ wn, row = wm * 32 + blk * 16 + i, odd = wm & 1;
      const float r = sc[1280 + row] * scl, m = sc[1536 + row] * scl;
      unsigned w[10];
#pragma unroll
      for (int j = 0; j < 5; j++) {
        w[2 * j] = pk2(c.acc[j][a][0] * r + m * cv[j][0], c.acc[j][a][1] * r + m * cv[j][1]);
        w[2 * j + 1] = pk2(c.acc[j][a][2] * r + m * cv[j][2], c.acc[j][a][3] * r + m * cv[j][3]);
      }
      const int rr = isq ? blk * 16 + i : 16 * (i >> 3) + 8 * blk + (i & 7);
      char* tb = isq ? ws + WS_Q + (head * 64 + rb * 4 + wm) * QT : ws + WS_KV + head * KVH + (rb * 2 + (wm >> 1)) * KVP + odd * KVT;
      ST16(tb + rr * 16, (u32x4{w[0], w[1], w[2], w[3]}));
      ST16(tb + 512 + rr * 16, (u32x4{w[4], w[5], w[6], w[7]}));
      ST8(tb + (isq ? 1024 + rr * 8 : 2 * KVT - odd * (KVT - 8) + rr * 16), (u32x2{w[8], w[9]}));
    }
  } else if (MODE == 1) {
    {
      const int odd = wm & 1, row = wm * 32 + 4 * g;
      const f32x4 r0 = *(const f32x4*)(sc + 1280 + row), r1 = *(const f32x4*)(sc + 1296 + row);
      const f32x4 m0 = *(const f32x4*)(sc + 1536 + row), m1 = *(const f32x4*)(sc + 1552 + row);
#pragma unroll
      for (int j = 0; j < 5; j++) {
        const int n = 16 * j + i, hl = (n * 52429) >> 20, d = n - hl * 20;
        const float cv = sc[1792 + wn * 96 + n];
        const f32x4 u = (wn ? c.acc[j][1] : c.acc[j][0]) * r0 + m0 * cv, v = (wn ? c.acc[j][0] : c.acc[j][1]) * r1 + m1 * cv;
        char* tb = smem + 8192 + wave * 5632 + hl * 1408 + g * VB + d * 16;
        *(u32x4*)tb = u32x4{pk2(u[0], u[1]), pk2(u[2], u[3]), pk2(v[0], v[1]), pk2(v[2], v[3])};
        if (j > 0 && d >= 18) { const unsigned k = (d == 18) ? 0x3f803f80u : 0u; *(u32x4*)(tb + 32) = u32x4{k, k, k, k}; }
      }
      asm volatile("s_waitcnt lgkmcnt(0)" ::: "memory");
      char* vb = ws + WS_KV + ((ct & 3) * 8 + wn * 4) * KVH + (rb * 2 + (wm >> 1)) * KVP + odd * KVT + 1024;
#pragma unroll
      for (int e = 0; e < 6; e++) {
        const int n = e * 64 + lane, hl = n / 88;
        if (n < 352) ST16(vb + hl * (KVH - 1408) + n * 16, *(const u32x4*)(smem + 8192 + wave * 5632 + n * 16));
      }
    }
  } else {
    f32x4 cv[6];
#pragma unroll
    for (int j = 0; j < 6; j++) cv[j] = *(const f32x4*)(sc + 1792 + wn * 96 + j * 16 + 4 * g);
#pragma unroll
    for (int a = 0; a < 2; a++) {
      const int sub = (wm & 1) * 2 + (a ^ wn);
      const float m = sc[1536 + wm * 32 + (a ^ wn) * 16 + i];
      unsigned w[6];
#pragma unroll
      for (int j = 0; j < 3; j++)
#pragma unroll
        for (int e = 0; e < 2; e++) {
          const unsigned gp = pk2(c.acc[j][a][2 * e] + m * cv[j][2 * e], c.acc[j][a][2 * e + 1] + m * cv[j][2 * e + 1]);
          const unsigned up = pk2(c.acc[j + 3][a][2 * e] + m * cv[j + 3][2 * e], c.acc[j + 3][a][2 * e + 1] + m * cv[j + 3][2 * e + 1]);
          w[2 * j + e] = pk2(bflo(gp) * bflo(up), bfhi(gp) * bfhi(up));
        }
      char* ab = ws + WS_A3 + (((rb * 2 + (wm >> 1)) * KS3 + 20 + ct * 3) * 4 + sub) * 1024;
      ST16(ab + wn * 8192 + lane * 16, (u32x4{w[0], w[1], w[2], w[3]}));
      ST8(ab + 4096 + wn * 512 + (g >> 1) * 256 + i * 16 + (g & 1) * 8, (u32x2{w[4], w[5]}));
    }
  }
}

__device__ __forceinline__ void retile_role(char* smem, const u16* __restrict__ wo, const u16* __restrict__ wd, char* __restrict__ ws,
                                            int idx, int tid, int wave) {
  const int lane = tid & 63, g = lane >> 4, q = (lane & 15) >> 2, p = lane & 3;
  const unsigned lds0 = (unsigned)(uintptr_t)smem;
  for (int k0 = idx; k0 < KS3; k0 += 64) {
    WAIT_BAR(0);
    for (int u = 0; u < 2; u++) {
      const int ks = k0 + 32 * u;
      if (ks < KS3) {
        const int orow = ks < 16 ? (2 * ks + (wave >> 2)) * 20 + (wave & 2) * 2 + (wave & 1) * 8 : (8 * (ks - 16) + wave) * 20 + 16;
        const char* src = (const char*)(ks < 20 ? wo + (size_t)orow * D_ : wd + (size_t)((ks - 20) * 32 + wave * 4) * D_) + lane * 16;
#pragma unroll
        for (int e = 0; e < 5; e++) glds16(src + e * 1024, lds0 + u * 40960 + wave * 5120 + e * 1024);
      }
    }
    WAIT_BAR(0);
    for (int u = 0; u < 2; u++) {
      const int ks = k0 + 32 * u;
      if (ks < KS3) {
        const bool nat = ks < 20 || ks % 3 == 0;
        const int row = nat ? 8 * g + q : 4 * g + q, sec = (nat ? 4 : 16) * 1280;
        lds_cptr b = (lds_cptr)smem + u * 40960 + row * 1280 + wave * 160;
#pragma unroll
        for (int t = 0; t < 5; t++)
          ST16(ws + WS_W3 + ((wave * KS3 + ks) * 5 + t) * 1024 + lane * 16, __builtin_bit_cast(u32x4, ldstr2(b + (t < 4 ? (t >> 1) * 64 + p * 16 + (t & 1) * 8 : 128 + p * 8), sec)));
      }
    }
  }
}

constexpr int AT_BUF = 4 * KVP;
constexpr int K2_LDS = 81920;

#define TOFF(t) (((t) >> 1) * KVP + ((t) & 1) * KVT)
struct KFrag { bf16x8 k1; s16x4 k2; };

__device__ __forceinline__ f32x16 qk_tile(const KFrag& k, bf16x8 q1, s16x4 q2) {
  f32x16 z = {};
  f32x16 s = MFMA32(k.k1, q1, z);
  return MFMA32X8(k.k2, q2, s);
}
__device__ __forceinline__ bf16x8 exp_pack(const f32x16& s, int m) {
  u32x4 w;
#pragma unroll
  for (int i = 0; i < 4; i++) w[i] = pk2(__builtin_amdgcn_exp2f(s[8 * m + 2 * i]), __builtin_amdgcn_exp2f(s[8 * m + 2 * i + 1]));
  return __builtin_bit_cast(bf16x8, w);
}
__device__ __forceinline__ float max16(const f32x16& s) {
  float a = fmaxf(s[0], s[1]);
#pragma unroll
  for (int i = 2; i < 16; i += 2) a = fmaxf(fmaxf(a, s[i]), s[i + 1]);
  return a;
}

#define AT_GROUP(MF, ACC, A, B, P, PJ, SJ) do { unsigned pa_, pb_; SBAR();                                                        \
    asm volatile(MF "\n\tv_cvt_pk_bf16_f32 %1, %3, %4\n\tv_cvt_pk_bf16_f32 %2, %5, %6\n\tv_exp_f32 %3, %9\n\tv_exp_f32 %4, %10\n\t"    \
                 "v_exp_f32 %5, %11\n\tv_exp_f32 %6, %12"                                                                          \
                 : ACC, "=&v"(pa_), "=&v"(pb_), "+v"(e[0]), "+v"(e[1]), "+v"(e[2]), "+v"(e[3])                                     \
                 : "v"(A), "v"(B), "v"(sa[SJ]), "v"(sa[SJ + 1]), "v"(sa[SJ + 2]), "v"(sa[SJ + 3]));                                \
    P[PJ] = pa_; P[PJ + 1] = pb_; SBAR(); } while (0)

__device__ __forceinline__ void attn_role(char* smem, char* __restrict__ ws, unsigned tag, int head, int qb, int tid, int wave PF_ARG) {
  const int lane = tid & 63, r = lane & 31, hh = lane >> 5;
  const unsigned lds0 = (unsigned)(uintptr_t)smem;
  const char* kvsrc = ws + WS_KV + (size_t)head * KVH + wave * 1024 + lane * 16;
  auto dma = [&](int c, int buf) {
    const char* src = kvsrc + (size_t)c * AT_BUF;
    const unsigned dst = lds0 + buf + wave * 1024;
    glds16d(src, dst);
    glds16d(src + 8192, dst + 8192);
    if (wave < 5) glds16d(src + 16384, dst + 16384);
  };
  if (wave == 0) poll(ws, FQ + (head >> 3) * 64 + min(lane, 47), tag);
  LGKM_BAR();
  STAMP(1, 0);
  dma(0, 0);
  const int qo = (int)WS_Q + (head * 64 + qb * 8 + wave) * QT;
  const bf16x8 q1 = LD16(lane * 16, qo);
  u32x2 q2u = LD8(1024 + r * 8, qo);
  if (hh) q2u = u32x2{0u, 0u};
  const int k1o = lane * 16, k2o = hh ? 1024 + 320 : 2 * KVT + r * 16, vo = 1024 + hh * VB + min(r, 21) * 16;
  lds_cptr lb = (lds_cptr)smem;
  LAS int* flag = (LAS int*)(lb + 3 * AT_BUF + 512);
  auto ldk = [&](int t) { KFrag k; k.k1 = lds128(lb + TOFF(t) + k1o); k.k2 = lds64(lb + (t >> 1) * KVP + (t & 1) * 8 + k2o); return k; };
  auto set_ref = [&](float m) {
    const float m0 = bfr(m), m1 = bfr(m - m0), m2 = bfr(m - m0 - m1);
    if (hh) q2u = u32x2{pk2(-m0, -m1), pk2(-m2, 0.f)};
  };
  WAIT_BAR(0);
  dma(1, AT_BUF);
  {
    KFrag k = ldk(0);
    set_ref(xhalf_max(max16(qk_tile(k, q1, __builtin_bit_cast(s16x4, q2u)))));
  }
  f32x16 o;
  STAMP(1, 1);
  for (int attempt = 0;; attempt++) {
    const s16x4 q2 = __builtin_bit_cast(s16x4, q2u);
    o = f32x16{};
    f32x16 sa;
    { KFrag k = ldk(0); sa = qk_tile(k, q1, q2); }
    bf16x8 k1 = lds128(lb + KVT + k1o), v1 = {}, v0;
    s16x8 k2p = __builtin_bit_cast(s16x8, lds128(lb + k2o));
    v0 = lds128(lb + vo);
    u32x4 p0 = {0u, 0u, 0u, 0u}, p1 = {0u, 0u, 0u, 0u};
    float e[4] = {0.f, 0.f, 0.f, 0.f};
    int cur = 0, nxt = AT_BUF;
    asm volatile("s_nop 15\n\ts_nop 7" : "+v"(sa), "+v"(o), "+v"(p1), "+v"(v1));
    for (int c = 0; c < 8; c++) {
      lds_cptr ck1 = lb + cur + k1o, ck2 = lb + cur + k2o, cv = lb + cur + vo;
      lds_cptr nk1 = lb + nxt + k1o, nk2 = lb + nxt + k2o, nv = lb + nxt + vo;
      const int nn = (nxt == 2 * AT_BUF) ? 0 : nxt + AT_BUF;
#pragma unroll
      for (int t = 0; t < 8; t++) {
        XL(8) if (t == 6) asm volatile("s_waitcnt vmcnt(0)\n\ts_barrier" ::: "memory");
        const int pc = (t + 2) & 7;
        const bool ld = pc < 3 && (pc < 2 ? c < 6 : (c > 0 && c < 7 && wave < 5));
        const char* src = kvsrc + (size_t)(t == 0 ? c + 1 : c + 2) * AT_BUF + pc * 8192;
        const unsigned dst = lds0 + (t == 0 ? nxt : nn) + wave * 1024 + pc * 8192;
        XL(8) if (ld && wave < 4) glds16d(src, dst);
        f32x16 sb;
        const s16x4 k2 = (t & 1) ? __builtin_shufflevector(k2p, k2p, 0, 1, 2, 3) : __builtin_shufflevector(k2p, k2p, 4, 5, 6, 7);
        AT_GROUP("v_mfma_f32_32x32x16_bf16 %0, %7, %8, 0", "=&v"(sb), k1, q1, p1, 2, 0);
        XL(1) k1 = lds128((t < 6 ? ck1 : nk1) + TOFF((t + 2) & 7));
        AT_GROUP("v_mfma_f32_32x32x16_bf16 %0, %7, %8, %0", "+v"(o), v1, p1, p0, 0, 4);
        XL(2) v1 = lds128(cv + TOFF(t) + 2 * VB);
        XL(8) if (ld && wave >= 4) glds16d(src, dst);
        AT_GROUP("v_mfma_f32_32x32x8_bf16 %0, %7, %8, %0", "+v"(sb), k2, q2, p0, 2, 8);
        XL(4) if (!(t & 1)) k2p = __builtin_bit_cast(s16x8, lds128((t < 6 ? ck2 : nk2) + TOFF((t + 2) & 7)));
        AT_GROUP("v_mfma_f32_32x32x16_bf16 %0, %7, %8, %0", "+v"(o), v0, p0, p1, 0, 12);
        XL(2) v0 = lds128((t < 7 ? cv : nv) + TOFF((t + 1) & 7));
        sa = sb;
      }
      cur = nxt; nxt = nn;
    }
    asm volatile("s_nop 7" : "+v"(o), "+v"(e[3]));
    p1[2] = pk2(e[0], e[1]); p1[3] = pk2(e[2], e[3]);
    o = MFMA32(v1, __builtin_bit_cast(bf16x8, p1), o);
    if (attempt == 1) break;
    bool ok = true;
#pragma unroll
    for (int i = 0; i < 12; i++) ok = ok && (fabsf(o[i]) < 1e30f || (hh && i > 8));
    LGKM_BAR();
    if (tid == 0) *flag = 0;
    LGKM_BAR();
    if (!ok) *flag = 1;
    LGKM_BAR();
    if (*flag == 0) break;
    if (hh) q2u = u32x2{0u, 0u};
    const s16x4 qz = __builtin_bit_cast(s16x4, q2u);
    float mx = -INFINITY;
    for (int c = 0; c < 8; c++) {
      LGKM_BAR();
      dma(c, 0);
      WAIT_BAR(0);
      for (int t = 0; t < 8; t++) { KFrag k = ldk(t); mx = fmaxf(mx, max16(qk_tile(k, q1, qz))); }
    }
    set_ref(xhalf_max(mx));
    LGKM_BAR();
    dma(0, 0); dma(1, AT_BUF);
    WAIT_BAR(0);
  }
  STAMP(1, 2);
  auto sw = __builtin_amdgcn_permlane32_swap(__float_as_uint(o[8]), __float_as_uint(o[8]), false, false);
  const float inv = 1.0f / __uint_as_float(sw[1]);
  const int s = qb * 256 + wave * 32 + r;
  char* ab = ws + WS_A3 + ((s >> 6) * KS3 * 4 + ((s >> 4) & 3)) * 1024;
  ST16(ab + (head >> 1) * 4096 + ((head & 1) * 2 + hh) * 256 + (s & 15) * 16,
       (u32x4{pk2(o[0] * inv, o[1] * inv), pk2(o[2] * inv, o[3] * inv), pk2(o[4] * inv, o[5] * inv), pk2(o[6] * inv, o[7] * inv)}));
  if (!hh) ST8(ab + (16 + (head >> 3)) * 4096 + (head & 7) * 128 + (s & 15) * 8, (u32x2{pk2(o[8] * inv, o[9] * inv), pk2(o[10] * inv, o[11] * inv)}));
  publish(ws, FO + qb * 32 + head, tag, tid);
}

__device__ __forceinline__ void down_role(char* smem, const u16* __restrict__ x, char* __restrict__ ws, u16* __restrict__ out, unsigned tag, int rb, int nb, int tid, int wave PF_ARG) {
  const int lane = tid & 63;
  if (wave == 0) poll(ws, lane < 18 ? FG + (rb >> 1) * 32 + lane : lane < 32 ? FQ + (rb >> 1) : FR - 32 + lane, tag);
  LGKM_BAR();
  STAMP(1, 0);
  int ap = (int)WS_A3 + (rb * KS3 + 20 + wave) * 4096, wp = (int)WS_W3 + (nb * KS3 + 20 + wave) * 5120;
  bf16x8 a[4], w[5];
#pragma unroll
  for (int t = 0; t < 5; t++) w[t] = LD16(lane * 16 + (t & 3) * 1024, wp + (t >> 2) * 4096);
#pragma unroll
  for (int i = 0; i < 4; i++) a[i] = LD16(lane * 16 + i * 1024, ap);
  f32x4 acc[20] = {};
  const int n = wave < 6 ? 7 : 6;
  for (int it = 1; it <= n; it++) {
    if (it < n) { ap += 8 * 4096; wp += 8 * 5120; }
#pragma unroll
    for (int i = 0; i < 4; i++) {
#pragma unroll
      for (int t = 0; t < 5; t++) acc[t * 4 + i] = MFMA16(w[t], a[i], acc[t * 4 + i]);
      SBAR();
      a[i] = LD16(lane * 16 + i * 1024, ap);
    }
#pragma unroll
    for (int t = 0; t < 5; t++) w[t] = LD16(lane * 16 + (t & 3) * 1024, wp + (t >> 2) * 4096);
  }
  STAMP(1, 2);
  __builtin_amdgcn_s_setprio(X_PB3);
  f32x4* red = (f32x4*)smem;
  const int rbk = wave & 3, kg = wave >> 2, g = lane >> 4, row = rb * 64 + rbk * 16 + (lane & 15);
  const size_t xo = (size_t)row * D_ + nb * 80;
  const u32x4 xv = *(const u32x4*)(x + xo + kg * 32 + g * 8);
  const u32x2 xw = *(const u32x2*)(x + xo + 64 + g * 4);
  const float r2 = __uint_as_float(__hip_atomic_load((gu32*)(ws + WS_ST) + row, RLX));
  f32x4 fin[3];
#pragma unroll
  for (int rd = 0; rd < 3; rd++) {
    if (rd) LGKM_BAR();
#pragma unroll
    for (int u = 0; u < (rd < 2 ? 8 : 4); u++) red[(wave * 8 + u) * 64 + lane] = acc[(rd < 2 ? 2 * (u >> 2) + rd : 4) * 4 + (u & 3)];
    LGKM_BAR();
    f32x4 v = red[(rd < 2 ? wave : rbk) * 64 + lane];
#pragma unroll
    for (int k = 1; k < 8; k++) v += red[(k * 8 + (rd < 2 ? wave : rbk)) * 64 + lane];
    const unsigned x0 = rd < 2 ? xv[2 * rd] : xw[0], x1 = rd < 2 ? xv[2 * rd + 1] : xw[1];
    fin[rd] = v * r2 + f32x4{bflo(x0), bfhi(x0), bflo(x1), bfhi(x1)};
    PIN(fin[rd]);
  }
  LGKM_BAR();
  STAMP(1, 4);
  const char* wo = ws + WS_W3 + ((size_t)nb * KS3 + kg * 10) * 5120 + lane * 16;
  const unsigned lds0 = (unsigned)(uintptr_t)smem;
#pragma unroll
  for (int e = 0; e < 10; e++) glds16d(wo + (10 + rbk * 10 + e) * 1024, lds0 + (wave * 10 + e) * 1024);
  bf16x8 wr[10];
#pragma unroll
  for (int e = 0; e < 10; e++) wr[e] = LD16(lane * 16 + (e & 3) * 1024, (int)WS_W3 + (nb * KS3 + kg * 10) * 5120 + (e >> 2) * 4096);
  f32x4 c[5];
  c[0] = kg ? f32x4{} : fin[0]; c[1] = kg ? f32x4{} : fin[1]; c[2] = kg ? fin[0] : f32x4{}; c[3] = kg ? fin[1] : f32x4{}; c[4] = kg ? f32x4{} : fin[2];
#pragma unroll
  for (int e = 0; e < 10; e++) PIN(wr[e]);
  asm volatile("" : "+v"(c[0]), "+v"(c[1]), "+v"(c[2]), "+v"(c[3]), "+v"(c[4]));
  STAMP(1, 5);
  if (wave == 0) poll(ws, FO + (rb >> 2) * 32 + (lane & 31), tag);
  WAIT_BAR(0);
  STAMP(1, 6);
  const int ob = (int)WS_A3 + ((rb * KS3 + kg * 10) * 4 + rbk) * 1024, o2 = kg ? g * 256 + (lane & 15) * 8 : lane * 16, o3 = o2 + (kg ? 128 : 8);
  bf16x8 of[10];
#pragma unroll
  for (int k = 0; k < 10; k++) {
    if (k < 6) of[k] = LD16(lane * 16, ob + k * 4096);
    else {
      const u32x2 lo = LD8(o2, ob + k * 4096), hi = LD8(o3, ob + k * 4096);
      of[k] = __builtin_bit_cast(bf16x8, u32x4{lo[0], lo[1], hi[0], hi[1]});
    }
  }
  SBAR();
  lds_cptr lw = (lds_cptr)smem + kg * 40960 + lane * 16;
#pragma unroll
  for (int k = 0; k < 10; k++) {
#pragma unroll
    for (int t = 0; t < 5; t++) c[t] = MFMA16(wr[(k & 1) * 5 + t], of[k], c[t]);
    asm volatile("" : "+v"(c[0]), "+v"(c[1]), "+v"(c[2]), "+v"(c[3]), "+v"(c[4]) :: "memory");
    if (k < 8)
#pragma unroll
      for (int t = 0; t < 5; t++) wr[(k & 1) * 5 + t] = lds128(lw + (k * 5 + t) * 1024);
    SBAR();
  }
  LGKM_BAR();
  red[(wave * 3) * 64 + lane] = kg ? c[0] : c[2];
  red[(wave * 3 + 1) * 64 + lane] = kg ? c[1] : c[3];
  if (kg) red[(wave * 3 + 2) * 64 + lane] = c[4];
  LGKM_BAR();
  const f32x4* pr = red + ((wave ^ 4) * 3) * 64 + lane;
  const f32x4 v0 = (kg ? c[2] : c[0]) + pr[0], v1 = (kg ? c[3] : c[1]) + pr[64];
  *(u32x4*)(out + xo + kg * 32 + g * 8) = u32x4{pk2(v0[0], v0[1]), pk2(v0[2], v0[3]), pk2(v1[0], v1[1]), pk2(v1[2], v1[3])};
  if (!kg) { const f32x4 v2 = c[4] + pr[128]; *(u32x2*)(out + xo + 64 + g * 4) = u32x2{pk2(v2[0], v2[1]), pk2(v2[2], v2[3])}; }
}

#define X_PRI1 2
#define X_PRI2 0
__global__ __launch_bounds__(512, 4) void fused_block(const u16* __restrict__ x, const u16* __restrict__ wqkv, const u16* __restrict__ wg,
                                                      const u16* __restrict__ wu, const u16* __restrict__ wo, const u16* __restrict__ wd,
                                                      char* __restrict__ ws, u16* __restrict__ out) {
  __shared__ __attribute__((aligned(16))) char smem[K2_LDS];
  const int tid = threadIdx.x;
  const int wave = __builtin_amdgcn_readfirstlane(tid >> 6);
  const int b = blockIdx.x, xcd = b & 7, o = (b >> 3) & 31, cg = xcd >> 1, t = o >> 3, rb = (xcd & 1) * 8 + (o & 7);
  PF_DECL; STAMP(0, 0);
  int slot;
  if (b >= 256) {
    const int u = min(tid - 320, 159);
    const char* pf = u < 0 ? (const char*)(x + (rb * 128 + t * 32) * D_) + tid * 128 : (const char*)(wqkv + (o + (u >> 3) * 32) * N1_ + cg * 480) + (u & 7) * 128;
    const unsigned dst = (unsigned)(uintptr_t)smem + 3 * STG + wave * 256;
    if (o == 0 && wave < 6) {
      const char* pc;
      asm volatile("s_getpc_b64 %0" : "=s"(pc));
      asm volatile("s_mov_b32 m0, %1\n\ts_nop 0\n\tglobal_load_lds_dword %0, off" :: "v"(pc + tid * 128), "s"(dst) : "memory", "m0");
    }
    if (o == 1 && wave < 6) {
      const char* pg = tid < 336 ? ws + tid * 65536 : (const char*)out + min(tid - 336, 39) * 65536;
      asm volatile("s_mov_b32 m0, %1\n\ts_nop 0\n\tglobal_load_lds_dword %0, off sc1" :: "v"(pg), "s"(dst) : "memory", "m0");
    }
    asm volatile("s_mov_b32 m0, %1\n\ts_nop 0\n\tglobal_load_lds_dword %0, off" :: "v"(pf), "s"(dst) : "memory", "m0");
    if (wave < 5) {
      pf = (const char*)((tid & 2 ? wu : wg) + ((o & 7) + (tid >> 2) * 8) * F_ + ((cg >> 1) * 9 + (cg & 1) * 5 + t) * 96) + (tid & 1) * 188;
      asm volatile("s_mov_b32 m0, %1\n\ts_nop 0\n\tglobal_load_lds_dword %0, off" :: "v"(pf), "s"(dst) : "memory", "m0");
    }
  }
  if (b < 256) __builtin_amdgcn_s_setprio(X_PRI1); else {
    __builtin_amdgcn_s_setprio(X_PB1);
    for (int i = 0; i < X_BS; i++) __builtin_amdgcn_s_sleep(32);
  }
  if (b < 256 && t < 3) {
    const int ct = cg * 3 + t;
    if (ct < 8) k1_tile<0>(smem, x, wqkv, wqkv, ws, rb, ct, tid, wave PF_PASS);
    else k1_tile<1>(smem, x, wqkv, wqkv, ws, rb, ct, tid, wave PF_PASS);
    slot = FQ + (ct & 3) * 64 + (ct >> 2) * 16 + rb;
  } else if (b >= 256 || !(cg & 1)) {
    const int ct = (cg >> 1) * 9 + (b < 256 ? 4 : (cg & 1) * 5 + t);
    k1_tile<2>(smem, x, wg, wu, ws, rb, ct, tid, wave PF_PASS);
    slot = FG + rb * 32 + ct;
  } else {
    retile_role(smem, wo, wd, ws, (cg >> 1) * 16 + rb, tid, wave);
    slot = FR + (cg >> 1) * 16 + rb;
  }
  STAMP(0, 4);
  const unsigned tag = __builtin_amdgcn_readfirstlane(__hip_atomic_load((gu32*)(ws + WS_FLG), RLX)) + 1u;
  publish(ws, slot, tag, tid);
  STAMP(0, 3);
  if (b < 256) {
    __builtin_amdgcn_s_setprio(X_PRI2);
    attn_role(smem, ws, tag, xcd * 4 + (o >> 3), o & 7, tid, wave PF_PASS);
  } else {
    __builtin_amdgcn_s_setprio(X_PB2);
    down_role(smem, x, ws, out, tag, (xcd & 3) * 8 + (o & 7), (xcd >> 2) * 4 + (o >> 3), tid, wave PF_PASS);
    if (b == 256) {
      poll(ws, FG + (tid >> 5) * 32 + min(tid & 31, 17), tag);
      publish(ws, 0, tag, tid);
    }
  }
  STAMP(1, 3);
}

extern "C" void kernel_launch(void* const* d_in, const int* in_sizes, int n_in, void* d_out, int out_size, void* d_ws, size_t ws_size,
                              hipStream_t stream) {
  fused_block<<<512, 512, 0, stream>>>((const u16*)d_in[0], (const u16*)d_in[1], (const u16*)d_in[4], (const u16*)d_in[5], (const u16*)d_in[2],
                                       (const u16*)d_in[6], (char*)d_ws, (u16*)d_out);
}
```

```cpp
#include <hip/hip_runtime.h>
#include <hip/hip_bf16.h>

typedef unsigned short u16;
typedef __attribute__((ext_vector_type(8))) __bf16 bf16x8;
typedef __attribute__((ext_vector_type(2))) __bf16 bf16x2;
typedef __attribute__((ext_vector_type(4))) short s16x4;
typedef __attribute__((ext_vector_type(8))) short s16x8;
typedef __attribute__((ext_vector_type(16))) float f32x16;
typedef __attribute__((ext_vector_type(4))) float f32x4;
typedef __attribute__((ext_vector_type(2))) float f32x2;
typedef __attribute__((ext_vector_type(4))) unsigned u32x4;
typedef __attribute__((ext_vector_type(2))) unsigned u32x2;
#define LAS __attribute__((address_space(3)))
typedef LAS const char* lds_cptr;
typedef __attribute__((address_space(1))) unsigned gu32;

constexpr int S_ = 2048, D_ = 640, F_ = 1728, N1_ = 1920;
constexpr int QT = 1280;
constexpr int VB = 352;
constexpr int KVT = 1024 + 4 * VB;
constexpr int KVP = 2 * KVT + 512;
constexpr int KVH = 32 * KVP;
constexpr int KS3 = 74;
constexpr size_t WS_ST = 0;
constexpr size_t WS_Q = WS_ST + 16384;
constexpr size_t WS_KV = WS_Q + (size_t)32 * 64 * QT;
constexpr size_t WS_A3 = WS_KV + (size_t)32 * KVH;
constexpr size_t WS_W3 = WS_A3 + (size_t)32 * KS3 * 4096;
constexpr size_t WS_FLG = WS_W3 + (size_t)8 * KS3 * 5120;
constexpr int FQ = 64, FG = 512, FR = 1024, FO = 1152;

#define MFMA32(a, b, c) __builtin_amdgcn_mfma_f32_32x32x16_bf16(a, b, c, 0, 0, 0)
#define MFMA32X8(a, b, c) __builtin_amdgcn_mfma_f32_32x32x8bf16_1k(a, b, c, 0, 0, 0)
#define MFMA16(a, b, c) __builtin_amdgcn_mfma_f32_16x16x32_bf16(a, b, c, 0, 0, 0)
#define SGB(m, n) __builtin_amdgcn_sched_group_barrier(m, n, 0)
#define WSR __builtin_amdgcn_make_buffer_rsrc(ws, 0, (int)WS_FLG, 0x00020000)
#define ST16(p, v) __builtin_amdgcn_raw_buffer_store_b128(v, WSR, (int)((p) - ws), 0, 16)
#define ST8(p, v) __builtin_amdgcn_raw_buffer_store_b64(v, WSR, (int)((p) - ws), 0, 16)
#define LD16(vo, so) __builtin_bit_cast(bf16x8, __builtin_amdgcn_raw_buffer_load_b128(WSR, vo, so, 16))
#define LD8(vo, so) __builtin_amdgcn_raw_buffer_load_b64(WSR, vo, so, 16)
#define RLX __ATOMIC_RELAXED, __HIP_MEMORY_SCOPE_AGENT
#define SBAR() __builtin_amdgcn_sched_barrier(0)
#define PIN(x) asm volatile("" : "+v"(x))
#define WAIT_BAR(N) asm volatile("s_waitcnt vmcnt(" #N ") lgkmcnt(0)\n\ts_barrier" ::: "memory")
#define LGKM_BAR() asm volatile("s_waitcnt lgkmcnt(0)\n\ts_barrier" ::: "memory")

#define X_PH 2
#define X_POLL 4
#define X_STG 4
#define X_PB1 1
#define X_PB2 0
#define X_PB3 0
#define X_BS 0
#define X_PF 2
#define X_PC 0
#define X_LM 0
#define XL(b) if (!(X_LM & b))
#define PF_DECL
#define PF_ARG
#define PF_PASS
#define STAMP(k, i)
__device__ __forceinline__ void glds16(const void* g, unsigned lds_base) {
  asm volatile("s_mov_b32 m0, %1\n\ts_nop 0\n\tglobal_load_lds_dwordx4 %0, off" :: "v"(g), "s"(lds_base) : "memory", "m0");
}
__device__ __forceinline__ void glds16s(const void* base, unsigned off, unsigned lds_base) {
  asm volatile("s_mov_b32 m0, %2\n\ts_nop 0\n\tglobal_load_lds_dwordx4 %0, %1" :: "v"(off), "s"(base), "s"(lds_base) : "memory", "m0");
}
__device__ __forceinline__ void glds16d(const void* g, unsigned lds_base) {
  asm volatile("s_mov_b32 m0, %1\n\ts_nop 0\n\tglobal_load_lds_dwordx4 %0, off sc1" :: "v"(g), "s"(lds_base) : "memory", "m0");
}
__device__ __forceinline__ void publish(char* ws, int slot, unsigned tag, int tid) {
  WAIT_BAR(0);
  if (tid == 0) __hip_atomic_store((gu32*)(ws + WS_FLG) + slot, tag, RLX);
}
__device__ __forceinline__ void poll(char* ws, int slot, unsigned tag) {
  for (int i = 0; i < (1 << 18); i++) {
    if (__all(__hip_atomic_load((gu32*)(ws + WS_FLG) + slot, RLX) == tag)) break;
    __builtin_amdgcn_s_sleep(X_POLL);
  }
}
__device__ __forceinline__ unsigned pk2(float lo, float hi) {
  f32x2 v = {lo, hi};
  return __builtin_bit_cast(unsigned, __builtin_convertvector(v, bf16x2));
}
__device__ __forceinline__ float bfr(float v) { return (float)(__bf16)v; }
__device__ __forceinline__ float bflo(unsigned u) { return __uint_as_float(u << 16); }
__device__ __forceinline__ float bfhi(unsigned u) { return __uint_as_float(u & 0xffff0000u); }
__device__ __forceinline__ bf16x8 lds128(lds_cptr p) { return *(const LAS bf16x8*)p; }
__device__ __forceinline__ s16x4 lds64(lds_cptr p) { return *(const LAS s16x4*)p; }
__device__ __forceinline__ bf16x8 ldstr2(lds_cptr p, int second) {
  s16x4 a = __builtin_amdgcn_ds_read_tr16_b64_v4i16((LAS s16x4*)p);
  s16x4 b = __builtin_amdgcn_ds_read_tr16_b64_v4i16((LAS s16x4*)(p + second));
  return __builtin_bit_cast(bf16x8, __builtin_shufflevector(a, b, 0, 1, 2, 3, 4, 5, 6, 7));
}
__device__ __forceinline__ float xhalf_max(float v) {
  auto r = __builtin_amdgcn_permlane32_swap(__float_as_uint(v), __float_as_uint(v), false, false);
  return fmaxf(__uint_as_float(r[0]), __uint_as_float(r[1]));
}
__device__ __forceinline__ void ln_stats(float sum, float ssq, float& mean_b, float& std_b) {
  float mean = sum * (1.0f / D_);
  float var = fmaxf(ssq * (1.0f / D_) - mean * mean, 0.0f);
  mean_b = bfr(mean);
  std_b = bfr(sqrtf(bfr(bfr(var) + 1e-5f)));
}

constexpr int STG = 12288, X0 = 4 * STG;
#define WAIT_BARN(N) asm volatile("s_waitcnt vmcnt(%0) lgkmcnt(0)\n\ts_barrier" :: "n"(N) : "memory")

template <int MODE>
struct K1Core {
  static constexpr int NB = MODE == 2 ? 6 : 5;
  f32x4 acc[NB][2], cs[2], rs, sq;
  bf16x8 sel;
  const u16 *xs, *wpa, *wpb;
  unsigned xl, wla, wlb;
  int wstep, xo, wo[5], wave;
  unsigned lds0;
  lds_cptr lb;
  static constexpr int ROW = MODE == 2 ? 192 : 320;
  __device__ __forceinline__ void issue_x(int h) {
    const unsigned dst = lds0 + X0 + (h & 2) * 8192 + wave * 2048;
    glds16s(xs + h * 32, xl, dst); glds16s(xs + h * 32 + 8 * D_, xl ^ 64, dst + 1024);
  }
  template <int PAR, int WM> __device__ __forceinline__ void issue_w(int h) {
    const unsigned dst = lds0 + (h & 3) * STG;
    glds16s(wpa + h * wstep, wla, dst + wave * 1024);
    if (MODE == 2 ? (WM >> 1) == PAR : WM == 2 * PAR) glds16s(wpb + h * wstep, wlb, dst + (8 + (wave & 3)) * 1024);
  }
  template <int PAR, int WM> __device__ __forceinline__ void step(int h, bool more) {
    int so = (h & 3) * STG, sx = (h & 2) * 8192;
    asm volatile("" : "+s"(so), "+s"(sx));
    lds_cptr st = lb + so;
    bf16x8 xf[2], wf[NB];
    xf[0] = lds128(lb + sx + (xo ^ (PAR * 64))); xf[1] = lds128(lb + sx + (xo ^ (PAR * 64) ^ 2048));
#pragma unroll
    for (int j = 0; j < NB; j++) wf[j] = ldstr2(st + (MODE == 2 ? wo[j % 3] + (j / 3) * 6144 : wo[j]), 4 * ROW);
    SBAR();
    if (more && (WM < X_STG)) { if (!PAR) issue_x(h + 2); issue_w<PAR ^ 1, WM>(h + 3); }
    SBAR();
    rs = MFMA16(sel, xf[0], rs);
    if (MODE != 2) sq = MFMA16(xf[0], xf[0], sq);
#pragma unroll
    for (int j = 0; j < NB; j++) {
#pragma unroll
      for (int a = 0; a < 2; a++) acc[j][a] = MODE == 1 ? MFMA16(xf[a], wf[j], acc[j][a]) : MFMA16(wf[j], xf[a], acc[j][a]);
      if ((j & 3) == WM) cs[j >> 2] = MFMA16(sel, wf[j], cs[j >> 2]);
    }
    SBAR();
#pragma unroll
    for (int j = 0; j < NB; j++) asm volatile("" : "+v"(acc[j][0]), "+v"(acc[j][1]));
    asm volatile("" : "+v"(cs[0]), "+v"(rs));
    if (WM + 4 < NB) PIN(cs[1]);
    if (MODE != 2) PIN(sq);
    if (more && !(WM < X_STG)) { if (!PAR) issue_x(h + 2); issue_w<PAR ^ 1, WM>(h + 3); }
  }
  template <int WM> __device__ __forceinline__ void run(char* ws PF_ARG) {
    constexpr int W2 = (MODE == 2 || !(WM & 1)) ? 3 : 2;
    issue_w<0, WM>(0); issue_x(0); issue_w<1, WM>(1); issue_w<0, WM>(2);
#pragma unroll
    for (int j = 0; j < NB; j++) acc[j][0] = acc[j][1] = f32x4{};
    cs[0] = cs[1] = rs = sq = f32x4{};
    for (int h = 0; h < 18; h += 2) {
      WAIT_BARN(W2); step<0, WM>(h, true);
      WAIT_BARN(W2 + 2); step<1, WM>(h + 1, h < 16);
    }
    WAIT_BAR(0); step<0, WM>(18, false); step<1, WM>(19, false);
  }
};

template <int MODE>
__device__ __forceinline__ void k1_tile(char* smem, const u16* __restrict__ x, const u16* __restrict__ w0, const u16* __restrict__ w1,
                                        char* __restrict__ ws, int rb, int ct, int tid, int wave PF_ARG) {
  constexpr int NB = K1Core<MODE>::NB, LD = MODE == 2 ? F_ : N1_;
  const int lane = tid & 63, i = lane & 15, g = lane >> 4, wm = wave >> 1, wn = wave & 1;
  K1Core<MODE> c;
  c.wave = wave; c.lds0 = (unsigned)(uintptr_t)smem; c.lb = (lds_cptr)smem; c.wstep = 32 * LD;
  c.xs = x + (rb * 128 + wave * 16) * D_;
  c.xl = (lane >> 3) * D_ * 2 + (((lane & 7) ^ g) << 4);
  {
    constexpr int CPR = K1Core<MODE>::ROW / 16;
    const int pa = MODE == 2 && wave >= 6 ? wave - 6 : wave, pb = MODE == 2 ? 2 + (wave & 3) : 8 + (wave & 1);
    const int ca = pa * 64 + lane, ra = ca / CPR, cb = pb * 64 + lane, rc = cb / CPR;
    c.wla = ra * LD * 2 + (((ca - ra * CPR) ^ ((ra >> 2) & 2)) << 4);
    c.wlb = rc * LD * 2 + (((cb - rc * CPR) ^ ((rc >> 2) & 2)) << 4);
    c.wpa = (wave >= 6 ? w1 : w0) + ct * (CPR * 8);
    c.wpb = w1 + ct * (CPR * 8);
  }
  c.xo = X0 + wave * 2048 + i * 128 + ((g ^ (i >> 1)) << 4);
  {
    const int q = i >> 2, p = lane & 3;
#pragma unroll
    for (int j = 0; j < 5; j++) {
      const int Q = MODE == 0 ? 5 * p + j + 20 * wn : MODE == 1 ? 4 * j + p + 20 * wn : 4 * (wn ? (j == 2 ? 3 : 4 + j) : j) + p;
      c.wo[j] = (8 * g + q) * K1Core<MODE>::ROW + ((Q ^ (4 * (g & 1))) << 3);
    }
    const unsigned v = i == 0 ? 0x3f803f80u : 0u;
    c.sel = __builtin_bit_cast(bf16x8, u32x4{v, v, v, v});
  }
  if (wm == 0) c.template run<0>(ws PF_PASS);
  else if (wm == 1) c.template run<1>(ws PF_PASS);
  else if (wm == 2) c.template run<2>(ws PF_PASS);
  else c.template run<3>(ws PF_PASS);
  STAMP(0, 2);
  float* sc = (float*)smem;
  if (g == 0) {
    sc[wave * 16 + i] = c.rs[0];
    sc[1792 + wn * 96 + wm * 16 + i] = c.cs[0][0];
    if (wm + 4 < NB) sc[1792 + wn * 96 + (wm + 4) * 16 + i] = c.cs[1][0];
  }
  if (MODE != 2 && g == (i >> 2)) {
    const float s01 = (i & 1) ? c.sq[1] : c.sq[0], s23 = (i & 1) ? c.sq[3] : c.sq[2];
    sc[256 + wave * 16 + i] = (i & 2) ? s23 : s01;
  }
  LGKM_BAR();
  if (tid < 128) {
    if (MODE == 2) {
      sc[1536 + tid] = -bfr(sc[tid] * (1.0f / D_));
    } else {
      float mean_b, std_b;
      ln_stats(sc[tid], sc[256 + tid], mean_b, std_b);
      const float r = 1.0f / std_b;
      sc[1280 + tid] = r; sc[1536 + tid] = -mean_b * r;
      if (ct == 0) __hip_atomic_store((gu32*)(ws + WS_ST) + rb * 128 + tid, __float_as_uint(r * r), RLX);
    }
  }
  LGKM_BAR();
  if (MODE == 0) {
    const bool isq = ct < 4;
    const float scl = isq ? 0.22360679774997896f * 1.4426950408889634f : 1.0f;
    const int head = (ct & 3) * 8 + wn * 4 + g;
    f32x4 cv[5];
#pragma unroll
    for (int j = 0; j < 5; j++) cv[j] = *(const f32x4*)(sc + 1792 + wn * 96 + j * 16 + 4 * g);
#pragma unroll
    for (int a = 0; a < 2; a++) {
      const int blk = a ^ wn, row = wm * 32 + blk * 16 + i, odd = wm & 1;
      const float r = sc[1280 + row] * scl, m = sc[1536 + row] * scl;
      unsigned w[10];
#pragma unroll
      for (int j = 0; j < 5; j++) {
        w[2 * j] = pk2(c.acc[j][a][0] * r + m * cv[j][0], c.acc[j][a][1] * r + m * cv[j][1]);
        w[2 * j + 1] = pk2(c.acc[j][a][2] * r + m * cv[j][2], c.acc[j][a][3] * r + m * cv[j][3]);
      }
      const int rr = isq ? blk * 16 + i : 16 * (i >> 3) + 8 * blk + (i & 7);
      char* tb = isq ? ws + WS_Q + (head * 64 + rb * 4 + wm) * QT : ws + WS_KV + head * KVH + (rb * 2 + (wm >> 1)) * KVP + odd * KVT;
      ST16(tb + rr * 16, (u32x4{w[0], w[1], w[2], w[3]}));
      ST16(tb + 512 + rr * 16, (u32x4{w[4], w[5], w[6], w[7]}));
      ST8(tb + (isq ? 1024 + rr * 8 : 2 * KVT - odd * (KVT - 8) + rr * 16), (u32x2{w[8], w[9]}));
    }
  } else if (MODE == 1) {
    {
      const int odd = wm & 1, row = wm * 32 + 4 * g;
      const f32x4 r0 = *(const f32x4*)(sc + 1280 + row), r1 = *(const f32x4*)(sc + 1296 + row);
      const f32x4 m0 = *(const f32x4*)(sc + 1536 + row), m1 = *(const f32x4*)(sc + 1552 + row);
#pragma unroll
      for (int j = 0; j < 5; j++) {
        const int n = 16 * j + i, hl = (n * 52429) >> 20, d = n - hl * 20;
        const float cv = sc[1792 + wn * 96 + n];
        const f32x4 u = (wn ? c.acc[j][1] : c.acc[j][0]) * r0 + m0 * cv, v = (wn ? c.acc[j][0] : c.acc[j][1]) * r1 + m1 * cv;
        char* tb = smem + 8192 + wave * 5632 + hl * 1408 + g * VB + d * 16;
        *(u32x4*)tb = u32x4{pk2(u[0], u[1]), pk2(u[2], u[3]), pk2(v[0], v[1]), pk2(v[2], v[3])};
        if (j > 0 && d >= 18) { const unsigned k = (d == 18) ? 0x3f803f80u : 0u; *(u32x4*)(tb + 32) = u32x4{k, k, k, k}; }
      }
      asm volatile("s_waitcnt lgkmcnt(0)" ::: "memory");
      char* vb = ws + WS_KV + ((ct & 3) * 8 + wn * 4) * KVH + (rb * 2 + (wm >> 1)) * KVP + odd * KVT + 1024;
#pragma unroll
      for (int e = 0; e < 6; e++) {
        const int n = e * 64 + lane, hl = n / 88;
        if (n < 352) ST16(vb + hl * (KVH - 1408) + n * 16, *(const u32x4*)(smem + 8192 + wave * 5632 + n * 16));
      }
    }
  } else {
    f32x4 cv[6];
#pragma unroll
    for (int j = 0; j < 6; j++) cv[j] = *(const f32x4*)(sc + 1792 + wn * 96 + j * 16 + 4 * g);
#pragma unroll
    for (int a = 0; a < 2; a++) {
      const int sub = (wm & 1) * 2 + (a ^ wn);
      const float m = sc[1536 + wm * 32 + (a ^ wn) * 16 + i];
      unsigned w[6];
#pragma unroll
      for (int j = 0; j < 3; j++)
#pragma unroll
        for (int e = 0; e < 2; e++) {
          const unsigned gp = pk2(c.acc[j][a][2 * e] + m * cv[j][2 * e], c.acc[j][a][2 * e + 1] + m * cv[j][2 * e + 1]);
          const unsigned up = pk2(c.acc[j + 3][a][2 * e] + m * cv[j + 3][2 * e], c.acc[j + 3][a][2 * e + 1] + m * cv[j + 3][2 * e + 1]);
          w[2 * j + e] = pk2(bflo(gp) * bflo(up), bfhi(gp) * bfhi(up));
        }
      char* ab = ws + WS_A3 + (((rb * 2 + (wm >> 1)) * KS3 + 20 + ct * 3) * 4 + sub) * 1024;
      ST16(ab + wn * 8192 + lane * 16, (u32x4{w[0], w[1], w[2], w[3]}));
      ST8(ab + 4096 + wn * 512 + (g >> 1) * 256 + i * 16 + (g & 1) * 8, (u32x2{w[4], w[5]}));
    }
  }
}

__device__ __forceinline__ void retile_role(char* smem, const u16* __restrict__ wo, const u16* __restrict__ wd, char* __restrict__ ws,
                                            int idx, int tid, int wave) {
  const int lane = tid & 63, g = lane >> 4, q = (lane & 15) >> 2, p = lane & 3;
  const unsigned lds0 = (unsigned)(uintptr_t)smem;
  for (int k0 = idx; k0 < KS3; k0 += 64) {
    WAIT_BAR(0);
    for (int u = 0; u < 2; u++) {
      const int ks = k0 + 32 * u;
      if (ks < KS3) {
        const int orow = ks < 16 ? (2 * ks + (wave >> 2)) * 20 + (wave & 2) * 2 + (wave & 1) * 8 : (8 * (ks - 16) + wave) * 20 + 16;
        const char* src = (const char*)(ks < 20 ? wo + (size_t)orow * D_ : wd + (size_t)((ks - 20) * 32 + wave * 4) * D_) + lane * 16;
#pragma unroll
        for (int e = 0; e < 5; e++) glds16(src + e * 1024, lds0 + u * 40960 + wave * 5120 + e * 1024);
      }
    }
    WAIT_BAR(0);
    for (int u = 0; u < 2; u++) {
      const int ks = k0 + 32 * u;
      if (ks < KS3) {
        const bool nat = ks < 20 || ks % 3 == 0;
        const int row = nat ? 8 * g + q : 4 * g + q, sec = (nat ? 4 : 16) * 1280;
        lds_cptr b = (lds_cptr)smem + u * 40960 + row * 1280 + wave * 160;
#pragma unroll
        for (int t = 0; t < 5; t++)
          ST16(ws + WS_W3 + ((wave * KS3 + ks) * 5 + t) * 1024 + lane * 16, __builtin_bit_cast(u32x4, ldstr2(b + (t < 4 ? (t >> 1) * 64 + p * 16 + (t & 1) * 8 : 128 + p * 8), sec)));
      }
    }
  }
}

constexpr int AT_BUF = 4 * KVP;
constexpr int K2_LDS = 81920;

#define TOFF(t) (((t) >> 1) * KVP + ((t) & 1) * KVT)
struct KFrag { bf16x8 k1; s16x4 k2; };

__device__ __forceinline__ f32x16 qk_tile(const KFrag& k, bf16x8 q1, s16x4 q2) {
  f32x16 z = {};
  f32x16 s = MFMA32(k.k1, q1, z);
  return MFMA32X8(k.k2, q2, s);
}
__device__ __forceinline__ bf16x8 exp_pack(const f32x16& s, int m) {
  u32x4 w;
#pragma unroll
  for (int i = 0; i < 4; i++) w[i] = pk2(__builtin_amdgcn_exp2f(s[8 * m + 2 * i]), __builtin_amdgcn_exp2f(s[8 * m + 2 * i + 1]));
  return __builtin_bit_cast(bf16x8, w);
}
__device__ __forceinline__ float max16(const f32x16& s) {
  float a = fmaxf(s[0], s[1]);
#pragma unroll
  for (int i = 2; i < 16; i += 2) a = fmaxf(fmaxf(a, s[i]), s[i + 1]);
  return a;
}

#define AT_GROUP(MF, ACC, A, B, P, PJ, SJ) do { unsigned pa_, pb_; SBAR();                                                        \
    asm volatile(MF "\n\tv_cvt_pk_bf16_f32 %1, %3, %4\n\tv_cvt_pk_bf16_f32 %2, %5, %6\n\tv_exp_f32 %3, %9\n\tv_exp_f32 %4, %10\n\t"    \
                 "v_exp_f32 %5, %11\n\tv_exp_f32 %6, %12"                                                                          \
                 : ACC, "=&v"(pa_), "=&v"(pb_), "+v"(e[0]), "+v"(e[1]), "+v"(e[2]), "+v"(e[3])                                     \
                 : "v"(A), "v"(B), "v"(sa[SJ]), "v"(sa[SJ + 1]), "v"(sa[SJ + 2]), "v"(sa[SJ + 3]));                                \
    P[PJ] = pa_; P[PJ + 1] = pb_; SBAR(); } while (0)

__device__ __forceinline__ void attn_role(char* smem, char* __restrict__ ws, unsigned tag, int head, int qb, int tid, int wave PF_ARG) {
  const int lane = tid & 63, r = lane & 31, hh = lane >> 5;
  const unsigned lds0 = (unsigned)(uintptr_t)smem;
  const char* kvsrc = ws + WS_KV + (size_t)head * KVH + wave * 1024 + lane * 16;
  auto dma = [&](int c, int buf) {
    const char* src = kvsrc + (size_t)c * AT_BUF;
    const unsigned dst = lds0 + buf + wave * 1024;
    glds16d(src, dst);
    glds16d(src + 8192, dst + 8192);
    if (wave < 5) glds16d(src + 16384, dst + 16384);
  };
  if (wave == 0) poll(ws, FQ + (head >> 3) * 64 + min(lane, 47), tag);
  LGKM_BAR();
  STAMP(1, 0);
  dma(0, 0);
  const int qo = (int)WS_Q + (head * 64 + qb * 8 + wave) * QT;
  const bf16x8 q1 = LD16(lane * 16, qo);
  u32x2 q2u = LD8(1024 + r * 8, qo);
  if (hh) q2u = u32x2{0u, 0u};
  const int k1o = lane * 16, k2o = hh ? 1024 + 320 : 2 * KVT + r * 16, vo = 1024 + hh * VB + min(r, 21) * 16;
  lds_cptr lb = (lds_cptr)smem;
  LAS int* flag = (LAS int*)(lb + 3 * AT_BUF + 512);
  auto ldk = [&](int t) { KFrag k; k.k1 = lds128(lb + TOFF(t) + k1o); k.k2 = lds64(lb + (t >> 1) * KVP + (t & 1) * 8 + k2o); return k; };
  auto set_ref = [&](float m) {
    const float m0 = bfr(m), m1 = bfr(m - m0), m2 = bfr(m - m0 - m1);
    if (hh) q2u = u32x2{pk2(-m0, -m1), pk2(-m2, 0.f)};
  };
  WAIT_BAR(0);
  dma(1, AT_BUF);
  {
    KFrag k = ldk(0);
    set_ref(xhalf_max(max16(qk_tile(k, q1, __builtin_bit_cast(s16x4, q2u)))));
  }
  f32x16 o;
  STAMP(1, 1);
  for (int attempt = 0;; attempt++) {
    const s16x4 q2 = __builtin_bit_cast(s16x4, q2u);
    o = f32x16{};
    f32x16 sa;
    { KFrag k = ldk(0); sa = qk_tile(k, q1, q2); }
    bf16x8 k1 = lds128(lb + KVT + k1o), v1 = {}, v0;
    s16x8 k2p = __builtin_bit_cast(s16x8, lds128(lb + k2o));
    v0 = lds128(lb + vo);
    u32x4 p0 = {0u, 0u, 0u, 0u}, p1 = {0u, 0u, 0u, 0u};
    float e[4] = {0.f, 0.f, 0.f, 0.f};
    int cur = 0, nxt = AT_BUF;
    asm volatile("s_nop 15\n\ts_nop 7" : "+v"(sa), "+v"(o), "+v"(p1), "+v"(v1));
    for (int c = 0; c < 8; c++) {
      lds_cptr ck1 = lb + cur + k1o, ck2 = lb + cur + k2o, cv = lb + cur + vo;
      lds_cptr nk1 = lb + nxt + k1o, nk2 = lb + nxt + k2o, nv = lb + nxt + vo;
      const int nn = (nxt == 2 * AT_BUF) ? 0 : nxt + AT_BUF;
#pragma unroll
      for (int t = 0; t < 8; t++) {
        XL(8) if (t == 6) asm volatile("s_waitcnt vmcnt(0)\n\ts_barrier" ::: "memory");
        const int pc = (t + 2) & 7;
        const bool ld = pc < 3 && (pc < 2 ? c < 6 : (c > 0 && c < 7 && wave < 5));
        const char* src = kvsrc + (size_t)(t == 0 ? c + 1 : c + 2) * AT_BUF + pc * 8192;
        const unsigned dst = lds0 + (t == 0 ? nxt : nn) + wave * 1024 + pc * 8192;
        XL(8) if (ld && wave < 4) glds16d(src, dst);
        f32x16 sb;
        const s16x4 k2 = (t & 1) ? __builtin_shufflevector(k2p, k2p, 0, 1, 2, 3) : __builtin_shufflevector(k2p, k2p, 4, 5, 6, 7);
        AT_GROUP("v_mfma_f32_32x32x16_bf16 %0, %7, %8, 0", "=&v"(sb), k1, q1, p1, 2, 0);
        XL(1) k1 = lds128((t < 6 ? ck1 : nk1) + TOFF((t + 2) & 7));
        AT_GROUP("v_mfma_f32_32x32x16_bf16 %0, %7, %8, %0", "+v"(o), v1, p1, p0, 0, 4);
        XL(2) v1 = lds128(cv + TOFF(t) + 2 * VB);
        XL(8) if (ld && wave >= 4) glds16d(src, dst);
        AT_GROUP("v_mfma_f32_32x32x8_bf16 %0, %7, %8, %0", "+v"(sb), k2, q2, p0, 2, 8);
        XL(4) if (!(t & 1)) k2p = __builtin_bit_cast(s16x8, lds128((t < 6 ? ck2 : nk2) + TOFF((t + 2) & 7)));
        AT_GROUP("v_mfma_f32_32x32x16_bf16 %0, %7, %8, %0", "+v"(o), v0, p0, p1, 0, 12);
        XL(2) v0 = lds128((t < 7 ? cv : nv) + TOFF((t + 1) & 7));
        sa = sb;
      }
      cur = nxt; nxt = nn;
    }
    asm volatile("s_nop 7" : "+v"(o), "+v"(e[3]));
    p1[2] = pk2(e[0], e[1]); p1[3] = pk2(e[2], e[3]);
    o = MFMA32(v1, __builtin_bit_cast(bf16x8, p1), o);
    if (attempt == 1) break;
    bool ok = true;
#pragma unroll
    for (int i = 0; i < 12; i++) ok = ok && (fabsf(o[i]) < 1e30f || (hh && i > 8));
    LGKM_BAR();
    if (tid == 0) *flag = 0;
    LGKM_BAR();
    if (!ok) *flag = 1;
    LGKM_BAR();
    if (*flag == 0) break;
    if (hh) q2u = u32x2{0u, 0u};
    const s16x4 qz = __builtin_bit_cast(s16x4, q2u);
    float mx = -INFINITY;
    for (int c = 0; c < 8; c++) {
      LGKM_BAR();
      dma(c, 0);
      WAIT_BAR(0);
      for (int t = 0; t < 8; t++) { KFrag k = ldk(t); mx = fmaxf(mx, max16(qk_tile(k, q1, qz))); }
    }
    set_ref(xhalf_max(mx));
    LGKM_BAR();
    dma(0, 0); dma(1, AT_BUF);
    WAIT_BAR(0);
  }
  STAMP(1, 2);
  auto sw = __builtin_amdgcn_permlane32_swap(__float_as_uint(o[8]), __float_as_uint(o[8]), false, false);
  const float inv = 1.0f / __uint_as_float(sw[1]);
  const int s = qb * 256 + wave * 32 + r;
  char* ab = ws + WS_A3 + ((s >> 6) * KS3 * 4 + ((s >> 4) & 3)) * 1024;
  ST16(ab + (head >> 1) * 4096 + ((head & 1) * 2 + hh) * 256 + (s & 15) * 16,
       (u32x4{pk2(o[0] * inv, o[1] * inv), pk2(o[2] * inv, o[3] * inv), pk2(o[4] * inv, o[5] * inv), pk2(o[6] * inv, o[7] * inv)}));
  if (!hh) ST8(ab + (16 + (head >> 3)) * 4096 + (head & 7) * 128 + (s & 15) * 8, (u32x2{pk2(o[8] * inv, o[9] * inv), pk2(o[10] * inv, o[11] * inv)}));
  publish(ws, FO + qb * 32 + head, tag, tid);
}

__device__ __forceinline__ void down_role(char* smem, const u16* __restrict__ x, char* __restrict__ ws, u16* __restrict__ out, unsigned tag, int rb, int nb, int tid, int wave PF_ARG) {
  const int lane = tid & 63;
  if (wave == 0) poll(ws, lane < 18 ? FG + (rb >> 1) * 32 + lane : lane < 32 ? FQ + (rb >> 1) : FR - 32 + lane, tag);
  LGKM_BAR();
  STAMP(1, 0);
  int ap = (int)WS_A3 + (rb * KS3 + 20 + wave) * 4096, wp = (int)WS_W3 + (nb * KS3 + 20 + wave) * 5120;
  bf16x8 a[4], w[5];
#pragma unroll
  for (int t = 0; t < 5; t++) w[t] = LD16(lane * 16 + (t & 3) * 1024, wp + (t >> 2) * 4096);
#pragma unroll
  for (int i = 0; i < 4; i++) a[i] = LD16(lane * 16 + i * 1024, ap);
  f32x4 acc[20] = {};
  const int n = wave < 6 ? 7 : 6;
  for (int it = 1; it <= n; it++) {
    if (it < n) { ap += 8 * 4096; wp += 8 * 5120; }
#pragma unroll
    for (int i = 0; i < 4; i++) {
#pragma unroll
      for (int t = 0; t < 5; t++) acc[t * 4 + i] = MFMA16(w[t], a[i], acc[t * 4 + i]);
      SBAR();
      a[i] = LD16(lane * 16 + i * 1024, ap);
    }
#pragma unroll
    for (int t = 0; t < 5; t++) w[t] = LD16(lane * 16 + (t & 3) * 1024, wp + (t >> 2) * 4096);
  }
  STAMP(1, 2);
  __builtin_amdgcn_s_setprio(X_PB3);
  f32x4* red = (f32x4*)smem;
  const int rbk = wave & 3, kg = wave >> 2, g = lane >> 4, row = rb * 64 + rbk * 16 + (lane & 15);
  const size_t xo = (size_t)row * D_ + nb * 80;
  const u32x4 xv = *(const u32x4*)(x + xo + kg * 32 + g * 8);
  const u32x2 xw = *(const u32x2*)(x + xo + 64 + g * 4);
  const float r2 = __uint_as_float(__hip_atomic_load((gu32*)(ws + WS_ST) + row, RLX));
  f32x4 fin[3];
#pragma unroll
  for (int rd = 0; rd < 3; rd++) {
    if (rd) LGKM_BAR();
#pragma unroll
    for (int u = 0; u < (rd < 2 ? 8 : 4); u++) red[(wave * 8 + u) * 64 + lane] = acc[(rd < 2 ? 2 * (u >> 2) + rd : 4) * 4 + (u & 3)];
    LGKM_BAR();
    f32x4 v = red[(rd < 2 ? wave : rbk) * 64 + lane];
#pragma unroll
    for (int k = 1; k < 8; k++) v += red[(k * 8 + (rd < 2 ? wave : rbk)) * 64 + lane];
    const unsigned x0 = rd < 2 ? xv[2 * rd] : xw[0], x1 = rd < 2 ? xv[2 * rd + 1] : xw[1];
    fin[rd] = v * r2 + f32x4{bflo(x0), bfhi(x0), bflo(x1), bfhi(x1)};
    PIN(fin[rd]);
  }
  LGKM_BAR();
  STAMP(1, 4);
  const char* wo = ws + WS_W3 + ((size_t)nb * KS3 + kg * 10) * 5120 + lane * 16;
  const unsigned lds0 = (unsigned)(uintptr_t)smem;
#pragma unroll
  for (int e = 0; e < 10; e++) glds16d(wo + (10 + rbk * 10 + e) * 1024, lds0 + (wave * 10 + e) * 1024);
  bf16x8 wr[10];
#pragma unroll
  for (int e = 0; e < 10; e++) wr[e] = LD16(lane * 16 + (e & 3) * 1024, (int)WS_W3 + (nb * KS3 + kg * 10) * 5120 + (e >> 2) * 4096);
  f32x4 c[5];
  c[0] = kg ? f32x4{} : fin[0]; c[1] = kg ? f32x4{} : fin[1]; c[2] = kg ? fin[0] : f32x4{}; c[3] = kg ? fin[1] : f32x4{}; c[4] = kg ? f32x4{} : fin[2];
#pragma unroll
  for (int e = 0; e < 10; e++) PIN(wr[e]);
  asm volatile("" : "+v"(c[0]), "+v"(c[1]), "+v"(c[2]), "+v"(c[3]), "+v"(c[4]));
  STAMP(1, 5);
  if (wave == 0) poll(ws, FO + (rb >> 2) * 32 + (lane & 31), tag);
  WAIT_BAR(0);
  STAMP(1, 6);
  const int ob = (int)WS_A3 + ((rb * KS3 + kg * 10) * 4 + rbk) * 1024, o2 = kg ? g * 256 + (lane & 15) * 8 : lane * 16, o3 = o2 + (kg ? 128 : 8);
  bf16x8 of[10];
#pragma unroll
  for (int k = 0; k < 10; k++) {
    if (k < 6) of[k] = LD16(lane * 16, ob + k * 4096);
    else {
      const u32x2 lo = LD8(o2, ob + k * 4096), hi = LD8(o3, ob + k * 4096);
      of[k] = __builtin_bit_cast(bf16x8, u32x4{lo[0], lo[1], hi[0], hi[1]});
    }
  }
  SBAR();
  lds_cptr lw = (lds_cptr)smem + kg * 40960 + lane * 16;
#pragma unroll
  for (int k = 0; k < 10; k++) {
#pragma unroll
    for (int t = 0; t < 5; t++) c[t] = MFMA16(wr[(k & 1) * 5 + t], of[k], c[t]);
    asm volatile("" : "+v"(c[0]), "+v"(c[1]), "+v"(c[2]), "+v"(c[3]), "+v"(c[4]) :: "memory");
    if (k < 8)
#pragma unroll
      for (int t = 0; t < 5; t++) wr[(k & 1) * 5 + t] = lds128(lw + (k * 5 + t) * 1024);
    SBAR();
  }
  LGKM_BAR();
  red[(wave * 3) * 64 + lane] = kg ? c[0] : c[2];
  red[(wave * 3 + 1) * 64 + lane] = kg ? c[1] : c[3];
  if (kg) red[(wave * 3 + 2) * 64 + lane] = c[4];
  LGKM_BAR();
  const f32x4* pr = red + ((wave ^ 4) * 3) * 64 + lane;
  const f32x4 v0 = (kg ? c[2] : c[0]) + pr[0], v1 = (kg ? c[3] : c[1]) + pr[64];
  *(u32x4*)(out + xo + kg * 32 + g * 8) = u32x4{pk2(v0[0], v0[1]), pk2(v0[2], v0[3]), pk2(v1[0], v1[1]), pk2(v1[2], v1[3])};
  if (!kg) { const f32x4 v2 = c[4] + pr[128]; *(u32x2*)(out + xo + 64 + g * 4) = u32x2{pk2(v2[0], v2[1]), pk2(v2[2], v2[3])}; }
}

#define X_PRI1 2
#define X_PRI2 0
__global__ __launch_bounds__(512, 4) void fused_block(const u16* __restrict__ x, const u16* __restrict__ wqkv, const u16* __restrict__ wg,
                                                      const u16* __restrict__ wu, const u16* __restrict__ wo, const u16* __restrict__ wd,
                                                      char* __restrict__ ws, u16* __restrict__ out) {
  __shared__ __attribute__((aligned(16))) char smem[K2_LDS];
  const int tid = threadIdx.x;
  const int wave = __builtin_amdgcn_readfirstlane(tid >> 6);
  const int b = blockIdx.x, xcd = b & 7, o = (b >> 3) & 31, cg = xcd >> 1, t = o >> 3, rb = (xcd & 1) * 8 + (o & 7);
  PF_DECL; STAMP(0, 0);
  int slot;
  if (b >= 256) {
    const int u = min(tid - 320, 159);
    const char* pf = u < 0 ? (const char*)(x + (rb * 128 + t * 32) * D_) + tid * 128 : (const char*)(wqkv + (o + (u >> 3) * 32) * N1_ + cg * 480) + (u & 7) * 128;
    const unsigned dst = (unsigned)(uintptr_t)smem + 3 * STG + wave * 256;
    asm volatile("s_mov_b32 m0, %1\n\ts_nop 0\n\tglobal_load_lds_dword %0, off" :: "v"(pf), "s"(dst) : "memory", "m0");
    if (wave < 5) {
      pf = (const char*)((tid & 2 ? wu : wg) + ((o & 7) + (tid >> 2) * 8) * F_ + ((cg >> 1) * 9 + (cg & 1) * 5 + t) * 96) + (tid & 1) * 188;
      asm volatile("s_mov_b32 m0, %1\n\ts_nop 0\n\tglobal_load_lds_dword %0, off" :: "v"(pf), "s"(dst) : "memory", "m0");
    }
  }
  if (b < 256) __builtin_amdgcn_s_setprio(X_PRI1); else {
    __builtin_amdgcn_s_setprio(X_PB1);
    for (int i = 0; i < X_BS; i++) __builtin_amdgcn_s_sleep(32);
  }
  if (b < 256 && t < 3) {
    const int ct = cg * 3 + t;
    if (ct < 8) k1_tile<0>(smem, x, wqkv, wqkv, ws, rb, ct, tid, wave PF_PASS);
    else k1_tile<1>(smem, x, wqkv, wqkv, ws, rb, ct, tid, wave PF_PASS);
    slot = FQ + (ct & 3) * 64 + (ct >> 2) * 16 + rb;
  } else if (b >= 256 || !(cg & 1)) {
    const int ct = (cg >> 1) * 9 + (b < 256 ? 4 : (cg & 1) * 5 + t);
    k1_tile<2>(smem, x, wg, wu, ws, rb, ct, tid, wave PF_PASS);
    slot = FG + rb * 32 + ct;
  } else {
    retile_role(smem, wo, wd, ws, (cg >> 1) * 16 + rb, tid, wave);
    slot = FR + (cg >> 1) * 16 + rb;
  }
  STAMP(0, 4);
  const unsigned tag = __builtin_amdgcn_readfirstlane(__hip_atomic_load((gu32*)(ws + WS_FLG), RLX)) + 1u;
  publish(ws, slot, tag, tid);
  STAMP(0, 3);
  if (b < 256) {
    __builtin_amdgcn_s_setprio(X_PRI2);
    attn_role(smem, ws, tag, xcd * 4 + (o >> 3), o & 7, tid, wave PF_PASS);
  } else {
    __builtin_amdgcn_s_setprio(X_PB2);
    down_role(smem, x, ws, out, tag, (xcd & 3) * 8 + (o & 7), (xcd >> 2) * 4 + (o >> 3), tid, wave PF_PASS);
    if (b == 256) {
      poll(ws, FG + (tid >> 5) * 32 + min(tid & 31, 17), tag);
      publish(ws, 0, tag, tid);
    }
  }
  STAMP(1, 3);
}

extern "C" void kernel_launch(void* const* d_in, const int* in_sizes, int n_in, void* d_out, int out_size, void* d_ws, size_t ws_size,
                              hipStream_t stream) {
  fused_block<<<512, 512, 0, stream>>>((const u16*)d_in[0], (const u16*)d_in[1], (const u16*)d_in[4], (const u16*)d_in[5], (const u16*)d_in[2],
                                       (const u16*)d_in[6], (char*)d_ws, (u16*)d_out);
}
```

```cpp
#include <hip/hip_runtime.h>
#include <hip/hip_bf16.h>

typedef unsigned short u16;
typedef __attribute__((ext_vector_type(8))) __bf16 bf16x8;
typedef __attribute__((ext_vector_type(2))) __bf16 bf16x2;
typedef __attribute__((ext_vector_type(4))) short s16x4;
typedef __attribute__((ext_vector_type(8))) short s16x8;
typedef __attribute__((ext_vector_type(16))) float f32x16;
typedef __attribute__((ext_vector_type(4))) float f32x4;
typedef __attribute__((ext_vector_type(2))) float f32x2;
typedef __attribute__((ext_vector_type(4))) unsigned u32x4;
typedef __attribute__((ext_vector_type(2))) unsigned u32x2;
#define LAS __attribute__((address_space(3)))
typedef LAS const char* lds_cptr;
typedef __attribute__((address_space(1))) unsigned gu32;

constexpr int S_ = 2048, D_ = 640, F_ = 1728, N1_ = 1920;
constexpr int QT = 1280;
constexpr int VB = 352;
constexpr int KVT = 1024 + 4 * VB;
constexpr int KVP = 2 * KVT + 512;
constexpr int KVH = 32 * KVP;
constexpr int KS3 = 74;
constexpr size_t WS_ST = 0;
constexpr size_t WS_Q = WS_ST + 16384;
constexpr size_t WS_KV = WS_Q + (size_t)32 * 64 * QT;
constexpr size_t WS_A3 = WS_KV + (size_t)32 * KVH;
constexpr size_t WS_W3 = WS_A3 + (size_t)32 * KS3 * 4096;
constexpr size_t WS_FLG = WS_W3 + (size_t)8 * KS3 * 5120;
constexpr int FREP = 1536;
constexpr int FQ = 64, FG = 512, FR = 1024, FO = 1152;

#define MFMA32(a, b, c) __builtin_amdgcn_mfma_f32_32x32x16_bf16(a, b, c, 0, 0, 0)
#define MFMA32X8(a, b, c) __builtin_amdgcn_mfma_f32_32x32x8bf16_1k(a, b, c, 0, 0, 0)
#define MFMA16(a, b, c) __builtin_amdgcn_mfma_f32_16x16x32_bf16(a, b, c, 0, 0, 0)
#define WSR __builtin_amdgcn_make_buffer_rsrc(ws, 0, (int)WS_FLG, 0x00020000)
#define ST16(p, v) __builtin_amdgcn_raw_buffer_store_b128(v, WSR, (int)((p) - ws), 0, 16)
#define ST8(p, v) __builtin_amdgcn_raw_buffer_store_b64(v, WSR, (int)((p) - ws), 0, 16)
#define LD16(vo, so) __builtin_bit_cast(bf16x8, __builtin_amdgcn_raw_buffer_load_b128(WSR, vo, so, 16))
#define LD8(vo, so) __builtin_amdgcn_raw_buffer_load_b64(WSR, vo, so, 16)
#define RLX __ATOMIC_RELAXED, __HIP_MEMORY_SCOPE_AGENT
#define SBAR() __builtin_amdgcn_sched_barrier(0)
#define PIN(x) asm volatile("" : "+v"(x))
#define WAIT_BAR(N) asm volatile("s_waitcnt vmcnt(" #N ") lgkmcnt(0)\n\ts_barrier" ::: "memory")
#define LGKM_BAR() asm volatile("s_waitcnt lgkmcnt(0)\n\ts_barrier" ::: "memory")

__device__ __forceinline__ void glds16(const void* g, unsigned lds_base) {
  asm volatile("s_mov_b32 m0, %1\n\ts_nop 0\n\tglobal_load_lds_dwordx4 %0, off" :: "v"(g), "s"(lds_base) : "memory", "m0");
}
__device__ __forceinline__ void glds16s(const void* base, unsigned off, unsigned lds_base) {
  asm volatile("s_mov_b32 m0, %2\n\ts_nop 0\n\tglobal_load_lds_dwordx4 %0, %1" :: "v"(off), "s"(base), "s"(lds_base) : "memory", "m0");
}
__device__ __forceinline__ void glds16d(const void* g, unsigned lds_base) {
  asm volatile("s_mov_b32 m0, %1\n\ts_nop 0\n\tglobal_load_lds_dwordx4 %0, off sc1" :: "v"(g), "s"(lds_base) : "memory", "m0");
}
__device__ __forceinline__ void publish(char* ws, int slot, unsigned tag, int tid) {
  WAIT_BAR(0);
  if (tid < 8) __hip_atomic_store((gu32*)(ws + WS_FLG) + tid * FREP + slot, tag, RLX);
}
__device__ __forceinline__ void poll(char* ws, int slot, unsigned tag) {
  for (int i = 0; i < (1 << 24); i++) {
    if (__all(__hip_atomic_load((gu32*)(ws + WS_FLG) + slot, RLX) == tag)) break;
    __builtin_amdgcn_s_sleep(4);
  }
}
__device__ __forceinline__ unsigned pk2(float lo, float hi) {
  f32x2 v = {lo, hi};
  return __builtin_bit_cast(unsigned, __builtin_convertvector(v, bf16x2));
}
__device__ __forceinline__ float bfr(float v) { return (float)(__bf16)v; }
__device__ __forceinline__ float bflo(unsigned u) { return __uint_as_float(u << 16); }
__device__ __forceinline__ float bfhi(unsigned u) { return __uint_as_float(u & 0xffff0000u); }
__device__ __forceinline__ bf16x8 lds128(lds_cptr p) { return *(const LAS bf16x8*)p; }
__device__ __forceinline__ s16x4 lds64(lds_cptr p) { return *(const LAS s16x4*)p; }
__device__ __forceinline__ bf16x8 ldstr2(lds_cptr p, int second) {
  s16x4 a = __builtin_amdgcn_ds_read_tr16_b64_v4i16((LAS s16x4*)p);
  s16x4 b = __builtin_amdgcn_ds_read_tr16_b64_v4i16((LAS s16x4*)(p + second));
  return __builtin_bit_cast(bf16x8, __builtin_shufflevector(a, b, 0, 1, 2, 3, 4, 5, 6, 7));
}
__device__ __forceinline__ float xhalf_max(float v) {
  auto r = __builtin_amdgcn_permlane32_swap(__float_as_uint(v), __float_as_uint(v), false, false);
  return fmaxf(__uint_as_float(r[0]), __uint_as_float(r[1]));
}
__device__ __forceinline__ void ln_stats(float sum, float ssq, float& mean_b, float& std_b) {
  float mean = sum * (1.0f / D_);
  float var = fmaxf(ssq * (1.0f / D_) - mean * mean, 0.0f);
  mean_b = bfr(mean);
  std_b = bfr(sqrtf(bfr(bfr(var) + 1e-5f)));
}

constexpr int STG = 12288, X0 = 4 * STG;
#define WAIT_BARN(N) asm volatile("s_waitcnt vmcnt(%0) lgkmcnt(0)\n\ts_barrier" :: "n"(N) : "memory")

template <int MODE>
struct K1Core {
  static constexpr int NB = MODE == 2 ? 6 : 5;
  f32x4 acc[NB][2], cs[2], rs, sq;
  bf16x8 sel;
  const u16 *xs, *wpa, *wpb;
  unsigned xl, wla, wlb;
  int wstep, xo, wo[5], wave;
  unsigned lds0;
  lds_cptr lb;
  static constexpr int ROW = MODE == 2 ? 192 : 320;
  __device__ __forceinline__ void issue_x(int h) {
    const unsigned dst = lds0 + X0 + (h & 2) * 8192 + wave * 2048;
    glds16s(xs + h * 32, xl, dst); glds16s(xs + h * 32 + 8 * D_, xl ^ 64, dst + 1024);
  }
  template <int PAR, int WM> __device__ __forceinline__ void issue_w(int h) {
    const unsigned dst = lds0 + (h & 3) * STG;
    glds16s(wpa + h * wstep, wla, dst + wave * 1024);
    if (MODE == 2 ? (WM >> 1) == PAR : WM == 2 * PAR) glds16s(wpb + h * wstep, wlb, dst + (8 + (wave & 3)) * 1024);
  }
  template <int PAR, int WM> __device__ __forceinline__ void step(int h, bool more) {
    int so = (h & 3) * STG, sx = (h & 2) * 8192;
    asm volatile("" : "+s"(so), "+s"(sx));
    lds_cptr st = lb + so;
    bf16x8 xf[2], wf[NB];
    xf[0] = lds128(lb + sx + (xo ^ (PAR * 64))); xf[1] = lds128(lb + sx + (xo ^ (PAR * 64) ^ 2048));
#pragma unroll
    for (int j = 0; j < NB; j++) wf[j] = ldstr2(st + (MODE == 2 ? wo[j % 3] + (j / 3) * 6144 : wo[j]), 4 * ROW);
    SBAR();
    if (more) { if (!PAR) issue_x(h + 2); issue_w<PAR ^ 1, WM>(h + 3); }
    SBAR();
    rs = MFMA16(sel, xf[0], rs);
    if (MODE != 2) sq = MFMA16(xf[0], xf[0], sq);
#pragma unroll
    for (int j = 0; j < NB; j++) {
#pragma unroll
      for (int a = 0; a < 2; a++) acc[j][a] = MODE == 1 ? MFMA16(xf[a], wf[j], acc[j][a]) : MFMA16(wf[j], xf[a], acc[j][a]);
      if ((j & 3) == WM) cs[j >> 2] = MFMA16(sel, wf[j], cs[j >> 2]);
    }
    SBAR();
#pragma unroll
    for (int j = 0; j < NB; j++) asm volatile("" : "+v"(acc[j][0]), "+v"(acc[j][1]));
    asm volatile("" : "+v"(cs[0]), "+v"(rs));
    if (WM + 4 < NB) PIN(cs[1]);
    if (MODE != 2) PIN(sq);
  }
  template <int WM> __device__ __forceinline__ void run() {
    constexpr int W2 = (MODE == 2 || !(WM & 1)) ? 3 : 2;
    issue_w<0, WM>(0); issue_x(0); issue_w<1, WM>(1); issue_w<0, WM>(2);
#pragma unroll
    for (int j = 0; j < NB; j++) acc[j][0] = acc[j][1] = f32x4{};
    cs[0] = cs[1] = rs = sq = f32x4{};
    for (int h = 0; h < 18; h += 2) {
      WAIT_BARN(W2); step<0, WM>(h, true);
      WAIT_BARN(W2 + 2); step<1, WM>(h + 1, h < 16);
    }
    WAIT_BAR(0); step<0, WM>(18, false); step<1, WM>(19, false);
  }
};

template <int MODE>
__device__ __forceinline__ void k1_tile(char* smem, const u16* __restrict__ x, const u16* __restrict__ w0, const u16* __restrict__ w1,
                                        char* __restrict__ ws, int rb, int ct, int tid, int wave) {
  constexpr int NB = K1Core<MODE>::NB, LD = MODE == 2 ? F_ : N1_;
  const int lane = tid & 63, i = lane & 15, g = lane >> 4, wm = wave >> 1, wn = wave & 1;
  K1Core<MODE> c;
  c.wave = wave; c.lds0 = (unsigned)(uintptr_t)smem; c.lb = (lds_cptr)smem; c.wstep = 32 * LD;
  c.xs = x + (rb * 128 + wave * 16) * D_;
  c.xl = (lane >> 3) * D_ * 2 + (((lane & 7) ^ g) << 4);
  {
    constexpr int CPR = K1Core<MODE>::ROW / 16;
    const int pa = MODE == 2 && wave >= 6 ? wave - 6 : wave, pb = MODE == 2 ? 2 + (wave & 3) : 8 + (wave & 1);
    const int ca = pa * 64 + lane, ra = ca / CPR, cb = pb * 64 + lane, rc = cb / CPR;
    c.wla = ra * LD * 2 + (((ca - ra * CPR) ^ ((ra >> 2) & 2)) << 4);
    c.wlb = rc * LD * 2 + (((cb - rc * CPR) ^ ((rc >> 2) & 2)) << 4);
    c.wpa = (wave >= 6 ? w1 : w0) + ct * (CPR * 8);
    c.wpb = w1 + ct * (CPR * 8);
  }
  c.xo = X0 + wave * 2048 + i * 128 + ((g ^ (i >> 1)) << 4);
  {
    const int q = i >> 2, p = lane & 3;
#pragma unroll
    for (int j = 0; j < 5; j++) {
      const int Q = MODE == 0 ? 5 * p + j + 20 * wn : MODE == 1 ? 4 * j + p + 20 * wn : 4 * (wn ? (j == 2 ? 3 : 4 + j) : j) + p;
      c.wo[j] = (8 * g + q) * K1Core<MODE>::ROW + ((Q ^ (4 * (g & 1))) << 3);
    }
    const unsigned v = i == 0 ? 0x3f803f80u : 0u;
    c.sel = __builtin_bit_cast(bf16x8, u32x4{v, v, v, v});
  }
  if (wm == 0) c.template run<0>();
  else if (wm == 1) c.template run<1>();
  else if (wm == 2) c.template run<2>();
  else c.template run<3>();
  float* sc = (float*)smem;
  if (g == 0) {
    sc[wave * 16 + i] = c.rs[0];
    sc[1792 + wn * 96 + wm * 16 + i] = c.cs[0][0];
    if (wm + 4 < NB) sc[1792 + wn * 96 + (wm + 4) * 16 + i] = c.cs[1][0];
  }
  if (MODE != 2 && g == (i >> 2)) {
    const float s01 = (i & 1) ? c.sq[1] : c.sq[0], s23 = (i & 1) ? c.sq[3] : c.sq[2];
    sc[256 + wave * 16 + i] = (i & 2) ? s23 : s01;
  }
  LGKM_BAR();
  if (tid < 128) {
    if (MODE == 2) {
      sc[1536 + tid] = -bfr(sc[tid] * (1.0f / D_));
    } else {
      float mean_b, std_b;
      ln_stats(sc[tid], sc[256 + tid], mean_b, std_b);
      const float r = 1.0f / std_b;
      sc[1280 + tid] = r; sc[1536 + tid] = -mean_b * r;
      if (ct == 0) __hip_atomic_store((gu32*)(ws + WS_ST) + rb * 128 + tid, __float_as_uint(r * r), RLX);
    }
  }
  LGKM_BAR();
  if (MODE == 0) {
    const bool isq = ct < 4;
    const float scl = isq ? 0.22360679774997896f * 1.4426950408889634f : 1.0f;
    const int head = (ct & 3) * 8 + wn * 4 + g;
    f32x4 cv[5];
#pragma unroll
    for (int j = 0; j < 5; j++) cv[j] = *(const f32x4*)(sc + 1792 + wn * 96 + j * 16 + 4 * g);
#pragma unroll
    for (int a = 0; a < 2; a++) {
      const int blk = a ^ wn, row = wm * 32 + blk * 16 + i, odd = wm & 1;
      const float r = sc[1280 + row] * scl, m = sc[1536 + row] * scl;
      unsigned w[10];
#pragma unroll
      for (int j = 0; j < 5; j++) {
        w[2 * j] = pk2(c.acc[j][a][0] * r + m * cv[j][0], c.acc[j][a][1] * r + m * cv[j][1]);
        w[2 * j + 1] = pk2(c.acc[j][a][2] * r + m * cv[j][2], c.acc[j][a][3] * r + m * cv[j][3]);
      }
      const int rr = isq ? blk * 16 + i : 16 * (i >> 3) + 8 * blk + (i & 7);
      char* tb = isq ? ws + WS_Q + (head * 64 + rb * 4 + wm) * QT : ws + WS_KV + head * KVH + (rb * 2 + (wm >> 1)) * KVP + odd * KVT;
      ST16(tb + rr * 16, (u32x4{w[0], w[1], w[2], w[3]}));
      ST16(tb + 512 + rr * 16, (u32x4{w[4], w[5], w[6], w[7]}));
      if (isq) ST8(tb + 1024 + rr * 8, (u32x2{w[8], w[9]}));
      else *(u32x2*)(smem + 8192 + (((wm >> 1) * 2 + wn) * 4 + g) * 512 + rr * 16 + odd * 8) = u32x2{w[8], w[9]};
    }
    if (!isq) {
      LGKM_BAR();
      const int hl = (wm & 1) * 2 + (lane >> 5);
      ST16(ws + WS_KV + ((ct & 3) * 8 + wn * 4 + hl) * KVH + (rb * 2 + (wm >> 1)) * KVP + 2 * KVT + (lane & 31) * 16,
           *(const u32x4*)(smem + 8192 + (((wm >> 1) * 2 + wn) * 4 + hl) * 512 + (lane & 31) * 16));
    }
  } else if (MODE == 1) {
    {
      const int odd = wm & 1, row = wm * 32 + 4 * g;
      const f32x4 r0 = *(const f32x4*)(sc + 1280 + row), r1 = *(const f32x4*)(sc + 1296 + row);
      const f32x4 m0 = *(const f32x4*)(sc + 1536 + row), m1 = *(const f32x4*)(sc + 1552 + row);
#pragma unroll
      for (int j = 0; j < 5; j++) {
        const int n = 16 * j + i, hl = (n * 52429) >> 20, d = n - hl * 20;
        const float cv = sc[1792 + wn * 96 + n];
        const f32x4 u = (wn ? c.acc[j][1] : c.acc[j][0]) * r0 + m0 * cv, v = (wn ? c.acc[j][0] : c.acc[j][1]) * r1 + m1 * cv;
        char* tb = smem + 8192 + wave * 5632 + hl * 1408 + g * VB + d * 16;
        *(u32x4*)tb = u32x4{pk2(u[0], u[1]), pk2(u[2], u[3]), pk2(v[0], v[1]), pk2(v[2], v[3])};
        if (j > 0 && d >= 18) { const unsigned k = (d == 18) ? 0x3f803f80u : 0u; *(u32x4*)(tb + 32) = u32x4{k, k, k, k}; }
      }
      asm volatile("s_waitcnt lgkmcnt(0)" ::: "memory");
      char* vb = ws + WS_KV + ((ct & 3) * 8 + wn * 4) * KVH + (rb * 2 + (wm >> 1)) * KVP + odd * KVT + 1024;
#pragma unroll
      for (int e = 0; e < 6; e++) {
        const int n = e * 64 + lane, hl = n / 88;
        if (n < 352) ST16(vb + hl * (KVH - 1408) + n * 16, *(const u32x4*)(smem + 8192 + wave * 5632 + n * 16));
      }
    }
  } else {
    f32x4 cv[6];
#pragma unroll
    for (int j = 0; j < 6; j++) cv[j] = *(const f32x4*)(sc + 1792 + wn * 96 + j * 16 + 4 * g);
#pragma unroll
    for (int a = 0; a < 2; a++) {
      const int sub = (wm & 1) * 2 + (a ^ wn);
      const float m = sc[1536 + wm * 32 + (a ^ wn) * 16 + i];
      unsigned w[6];
#pragma unroll
      for (int j = 0; j < 3; j++)
#pragma unroll
        for (int e = 0; e < 2; e++) {
          const unsigned gp = pk2(c.acc[j][a][2 * e] + m * cv[j][2 * e], c.acc[j][a][2 * e + 1] + m * cv[j][2 * e + 1]);
          const unsigned up = pk2(c.acc[j + 3][a][2 * e] + m * cv[j + 3][2 * e], c.acc[j + 3][a][2 * e + 1] + m * cv[j + 3][2 * e + 1]);
          w[2 * j + e] = pk2(bflo(gp) * bflo(up), bfhi(gp) * bfhi(up));
        }
      char* ab = ws + WS_A3 + (((rb * 2 + (wm >> 1)) * KS3 + 20 + ct * 3) * 4 + sub) * 1024;
      ST16(ab + wn * 8192 + lane * 16, (u32x4{w[0], w[1], w[2], w[3]}));
      ST8(ab + 4096 + wn * 512 + (g >> 1) * 256 + i * 16 + (g & 1) * 8, (u32x2{w[4], w[5]}));
    }
  }
}

__device__ __forceinline__ void retile_role(char* smem, const u16* __restrict__ wo, const u16* __restrict__ wd, char* __restrict__ ws,
                                            int idx, int tid, int wave) {
  const int lane = tid & 63, g = lane >> 4, q = (lane & 15) >> 2, p = lane & 3;
  const unsigned lds0 = (unsigned)(uintptr_t)smem;
  for (int k0 = idx; k0 < KS3; k0 += 64) {
    WAIT_BAR(0);
    for (int u = 0; u < 2; u++) {
      const int ks = k0 + 32 * u;
      if (ks < KS3) {
        const int orow = ks < 16 ? (2 * ks + (wave >> 2)) * 20 + (wave & 2) * 2 + (wave & 1) * 8 : (8 * (ks - 16) + wave) * 20 + 16;
        const char* src = (const char*)(ks < 20 ? wo + (size_t)orow * D_ : wd + (size_t)((ks - 20) * 32 + wave * 4) * D_) + lane * 16;
#pragma unroll
        for (int e = 0; e < 5; e++) glds16(src + e * 1024, lds0 + u * 40960 + wave * 5120 + e * 1024);
      }
    }
    WAIT_BAR(0);
    for (int u = 0; u < 2; u++) {
      const int ks = k0 + 32 * u;
      if (ks < KS3) {
        const bool nat = ks < 20 || ks % 3 == 0;
        const int row = nat ? 8 * g + q : 4 * g + q, sec = (nat ? 4 : 16) * 1280;
        lds_cptr b = (lds_cptr)smem + u * 40960 + row * 1280 + wave * 160;
#pragma unroll
        for (int t = 0; t < 5; t++)
          ST16(ws + WS_W3 + ((wave * KS3 + ks) * 5 + t) * 1024 + lane * 16, __builtin_bit_cast(u32x4, ldstr2(b + (t < 4 ? (t >> 1) * 64 + p * 16 + (t & 1) * 8 : 128 + p * 8), sec)));
      }
    }
  }
}

constexpr int AT_BUF = 4 * KVP;
constexpr int K2_LDS = 81920;

#define TOFF(t) (((t) >> 1) * KVP + ((t) & 1) * KVT)
struct KFrag { bf16x8 k1; s16x4 k2; };

__device__ __forceinline__ f32x16 qk_tile(const KFrag& k, bf16x8 q1, s16x4 q2) {
  f32x16 z = {};
  f32x16 s = MFMA32(k.k1, q1, z);
  return MFMA32X8(k.k2, q2, s);
}
__device__ __forceinline__ bf16x8 exp_pack(const f32x16& s, int m) {
  u32x4 w;
#pragma unroll
  for (int i = 0; i < 4; i++) w[i] = pk2(__builtin_amdgcn_exp2f(s[8 * m + 2 * i]), __builtin_amdgcn_exp2f(s[8 * m + 2 * i + 1]));
  return __builtin_bit_cast(bf16x8, w);
}
__device__ __forceinline__ float max16(const f32x16& s) {
  float a = fmaxf(s[0], s[1]);
#pragma unroll
  for (int i = 2; i < 16; i += 2) a = fmaxf(fmaxf(a, s[i]), s[i + 1]);
  return a;
}

#define AT_GROUP(MF, ACC, A, B, P, PJ, SJ) do { unsigned pa_, pb_; SBAR();                                                        \
    asm volatile(MF "\n\tv_cvt_pk_bf16_f32 %1, %3, %4\n\tv_cvt_pk_bf16_f32 %2, %5, %6\n\tv_exp_f32 %3, %9\n\tv_exp_f32 %4, %10\n\t"    \
                 "v_exp_f32 %5, %11\n\tv_exp_f32 %6, %12"                                                                          \
                 : ACC, "=&v"(pa_), "=&v"(pb_), "+v"(e[0]), "+v"(e[1]), "+v"(e[2]), "+v"(e[3])                                     \
                 : "v"(A), "v"(B), "v"(sa[SJ]), "v"(sa[SJ + 1]), "v"(sa[SJ + 2]), "v"(sa[SJ + 3]));                                \
    P[PJ] = pa_; P[PJ + 1] = pb_; SBAR(); } while (0)

__device__ __forceinline__ void attn_role(char* smem, char* __restrict__ ws, unsigned tag, int head, int qb, int tid, int wave) {
  const int lane = tid & 63, r = lane & 31, hh = lane >> 5;
  const unsigned lds0 = (unsigned)(uintptr_t)smem;
  const char* kvsrc = ws + WS_KV + (size_t)head * KVH + wave * 1024 + lane * 16;
  auto dma = [&](int c, int buf) {
    const char* src = kvsrc + (size_t)c * AT_BUF;
    const unsigned dst = lds0 + buf + wave * 1024;
    glds16d(src, dst);
    glds16d(src + 8192, dst + 8192);
    if (wave < 5) glds16d(src + 16384, dst + 16384);
  };
  if (wave == 0) poll(ws, qb * FREP + FQ + (head >> 3) * 64 + min(lane, 47), tag);
  LGKM_BAR();
  dma(0, 0);
  const int qo = (int)WS_Q + (head * 64 + qb * 8 + wave) * QT;
  const bf16x8 q1 = LD16(lane * 16, qo);
  u32x2 q2u = LD8(1024 + r * 8, qo);
  if (hh) q2u = u32x2{0u, 0u};
  const int k1o = lane * 16, k2o = hh ? 1024 + 320 : 2 * KVT + r * 16, vo = 1024 + hh * VB + min(r, 21) * 16;
  lds_cptr lb = (lds_cptr)smem;
  LAS int* flag = (LAS int*)(lb + 3 * AT_BUF + 512);
  auto ldk = [&](int t) { KFrag k; k.k1 = lds128(lb + TOFF(t) + k1o); k.k2 = lds64(lb + (t >> 1) * KVP + (t & 1) * 8 + k2o); return k; };
  auto set_ref = [&](float m) {
    const float m0 = bfr(m), m1 = bfr(m - m0), m2 = bfr(m - m0 - m1);
    if (hh) q2u = u32x2{pk2(-m0, -m1), pk2(-m2, 0.f)};
  };
  WAIT_BAR(0);
  dma(1, AT_BUF);
  {
    KFrag k = ldk(0);
    set_ref(xhalf_max(max16(qk_tile(k, q1, __builtin_bit_cast(s16x4, q2u)))));
  }
  f32x16 o;
  for (int attempt = 0;; attempt++) {
    const s16x4 q2 = __builtin_bit_cast(s16x4, q2u);
    o = f32x16{};
    f32x16 sa;
    { KFrag k = ldk(0); sa = qk_tile(k, q1, q2); }
    bf16x8 k1 = lds128(lb + KVT + k1o), v1 = {}, v0;
    s16x8 k2p = __builtin_bit_cast(s16x8, lds128(lb + k2o));
    v0 = lds128(lb + vo);
    u32x4 p0 = {0u, 0u, 0u, 0u}, p1 = {0u, 0u, 0u, 0u};
    float e[4] = {0.f, 0.f, 0.f, 0.f};
    int cur = 0, nxt = AT_BUF;
    asm volatile("s_nop 15\n\ts_nop 7" : "+v"(sa), "+v"(o), "+v"(p1), "+v"(v1));
    for (int c = 0; c < 8; c++) {
      lds_cptr ck1 = lb + cur + k1o, ck2 = lb + cur + k2o, cv = lb + cur + vo;
      lds_cptr nk1 = lb + nxt + k1o, nk2 = lb + nxt + k2o, nv = lb + nxt + vo;
      const int nn = (nxt == 2 * AT_BUF) ? 0 : nxt + AT_BUF;
#pragma unroll
      for (int t = 0; t < 8; t++) {
        if (t == 6) asm volatile("s_waitcnt vmcnt(0)\n\ts_barrier" ::: "memory");
        const int pc = (t + 2) & 7;
        const bool ld = pc < 3 && (pc < 2 ? c < 6 : (c > 0 && c < 7 && wave < 5));
        const char* src = kvsrc + (size_t)(t == 0 ? c + 1 : c + 2) * AT_BUF + pc * 8192;
        const unsigned dst = lds0 + (t == 0 ? nxt : nn) + wave * 1024 + pc * 8192;
        if (ld && wave < 4) glds16d(src, dst);
        f32x16 sb;
        const s16x4 k2 = (t & 1) ? __builtin_shufflevector(k2p, k2p, 0, 1, 2, 3) : __builtin_shufflevector(k2p, k2p, 4, 5, 6, 7);
        AT_GROUP("v_mfma_f32_32x32x16_bf16 %0, %7, %8, 0", "=&v"(sb), k1, q1, p1, 2, 0);
        k1 = lds128((t < 6 ? ck1 : nk1) + TOFF((t + 2) & 7));
        AT_GROUP("v_mfma_f32_32x32x16_bf16 %0, %7, %8, %0", "+v"(o), v1, p1, p0, 0, 4);
        v1 = lds128(cv + TOFF(t) + 2 * VB);
        if (ld && wave >= 4) glds16d(src, dst);
        AT_GROUP("v_mfma_f32_32x32x8_bf16 %0, %7, %8, %0", "+v"(sb), k2, q2, p0, 2, 8);
        if (!(t & 1)) k2p = __builtin_bit_cast(s16x8, lds128((t < 6 ? ck2 : nk2) + TOFF((t + 2) & 7)));
        AT_GROUP("v_mfma_f32_32x32x16_bf16 %0, %7, %8, %0", "+v"(o), v0, p0, p1, 0, 12);
        v0 = lds128((t < 7 ? cv : nv) + TOFF((t + 1) & 7));
        sa = sb;
      }
      cur = nxt; nxt = nn;
    }
    asm volatile("s_nop 7" : "+v"(o), "+v"(e[3]));
    p1[2] = pk2(e[0], e[1]); p1[3] = pk2(e[2], e[3]);
    o = MFMA32(v1, __builtin_bit_cast(bf16x8, p1), o);
    if (attempt == 1) break;
    bool ok = true;
#pragma unroll
    for (int i = 0; i < 12; i++) ok = ok && (fabsf(o[i]) < 1e30f || (hh && i > 8));
    LGKM_BAR();
    if (tid == 0) *flag = 0;
    LGKM_BAR();
    if (!ok) *flag = 1;
    LGKM_BAR();
    if (*flag == 0) break;
    if (hh) q2u = u32x2{0u, 0u};
    const s16x4 qz = __builtin_bit_cast(s16x4, q2u);
    float mx = -INFINITY;
    for (int c = 0; c < 8; c++) {
      LGKM_BAR();
      dma(c, 0);
      WAIT_BAR(0);
      for (int t = 0; t < 8; t++) { KFrag k = ldk(t); mx = fmaxf(mx, max16(qk_tile(k, q1, qz))); }
    }
    set_ref(xhalf_max(mx));
    LGKM_BAR();
    dma(0, 0); dma(1, AT_BUF);
    WAIT_BAR(0);
  }
  auto sw = __builtin_amdgcn_permlane32_swap(__float_as_uint(o[8]), __float_as_uint(o[8]), false, false);
  const float inv = 1.0f / __uint_as_float(sw[1]);
  const int s = qb * 256 + wave * 32 + r;
  char* ab = ws + WS_A3 + ((s >> 6) * KS3 * 4 + ((s >> 4) & 3)) * 1024;
  ST16(ab + (head >> 1) * 4096 + ((head & 1) * 2 + hh) * 256 + (s & 15) * 16,
       (u32x4{pk2(o[0] * inv, o[1] * inv), pk2(o[2] * inv, o[3] * inv), pk2(o[4] * inv, o[5] * inv), pk2(o[6] * inv, o[7] * inv)}));
  if (!hh) ST8(ab + (16 + (head >> 3)) * 4096 + (head & 7) * 128 + (s & 15) * 8, (u32x2{pk2(o[8] * inv, o[9] * inv), pk2(o[10] * inv, o[11] * inv)}));
  publish(ws, FO + qb * 32 + head, tag, tid);
}

__device__ __forceinline__ void down_role(char* smem, const u16* __restrict__ x, char* __restrict__ ws, u16* __restrict__ out, unsigned tag, int rb, int nb, int tid, int wave) {
  const int lane = tid & 63;
  if (wave == 0) poll(ws, nb * FREP + (lane < 18 ? FG + (rb >> 1) * 32 + lane : lane < 32 ? FQ + (rb >> 1) : FR - 32 + lane), tag);
  LGKM_BAR();
  int ap = (int)WS_A3 + (rb * KS3 + 20 + wave) * 4096, wp = (int)WS_W3 + (nb * KS3 + 20 + wave) * 5120;
  bf16x8 a[4], w[5];
#pragma unroll
  for (int t = 0; t < 5; t++) w[t] = LD16(lane * 16 + (t & 3) * 1024, wp + (t >> 2) * 4096);
#pragma unroll
  for (int i = 0; i < 4; i++) a[i] = LD16(lane * 16 + i * 1024, ap);
  f32x4 acc[20] = {};
  const int n = wave < 6 ? 7 : 6;
  for (int it = 1; it <= n; it++) {
    if (it < n) { ap += 8 * 4096; wp += 8 * 5120; }
#pragma unroll
    for (int i = 0; i < 4; i++) {
#pragma unroll
      for (int t = 0; t < 5; t++) acc[t * 4 + i] = MFMA16(w[t], a[i], acc[t * 4 + i]);
      SBAR();
      a[i] = LD16(lane * 16 + i * 1024, ap);
    }
#pragma unroll
    for (int t = 0; t < 5; t++) w[t] = LD16(lane * 16 + (t & 3) * 1024, wp + (t >> 2) * 4096);
  }
  f32x4* red = (f32x4*)smem;
  const int rbk = wave & 3, kg = wave >> 2, g = lane >> 4, row = rb * 64 + rbk * 16 + (lane & 15);
  const size_t xo = (size_t)row * D_ + nb * 80;
  const u32x4 xv = *(const u32x4*)(x + xo + kg * 32 + g * 8);
  const u32x2 xw = *(const u32x2*)(x + xo + 64 + g * 4);
  const float r2 = __uint_as_float(__hip_atomic_load((gu32*)(ws + WS_ST) + row, RLX));
  f32x4 fin[3];
#pragma unroll
  for (int rd = 0; rd < 3; rd++) {
    if (rd) LGKM_BAR();
#pragma unroll
    for (int u = 0; u < (rd < 2 ? 8 : 4); u++) red[(wave * 8 + u) * 64 + lane] = acc[(rd < 2 ? 2 * (u >> 2) + rd : 4) * 4 + (u & 3)];
    LGKM_BAR();
    f32x4 v = red[(rd < 2 ? wave : rbk) * 64 + lane];
#pragma unroll
    for (int k = 1; k < 8; k++) v += red[(k * 8 + (rd < 2 ? wave : rbk)) * 64 + lane];
    const unsigned x0 = rd < 2 ? xv[2 * rd] : xw[0], x1 = rd < 2 ? xv[2 * rd + 1] : xw[1];
    fin[rd] = v * r2 + f32x4{bflo(x0), bfhi(x0), bflo(x1), bfhi(x1)};
    PIN(fin[rd]);
  }
  LGKM_BAR();
  const char* wo = ws + WS_W3 + ((size_t)nb * KS3 + kg * 10) * 5120 + lane * 16;
  const unsigned lds0 = (unsigned)(uintptr_t)smem;
#pragma unroll
  for (int e = 0; e < 10; e++) glds16d(wo + (10 + rbk * 10 + e) * 1024, lds0 + (wave * 10 + e) * 1024);
  bf16x8 wr[10];
#pragma unroll
  for (int e = 0; e < 10; e++) wr[e] = LD16(lane * 16 + (e & 3) * 1024, (int)WS_W3 + (nb * KS3 + kg * 10) * 5120 + (e >> 2) * 4096);
  f32x4 c[5];
  c[0] = kg ? f32x4{} : fin[0]; c[1] = kg ? f32x4{} : fin[1]; c[2] = kg ? fin[0] : f32x4{}; c[3] = kg ? fin[1] : f32x4{}; c[4] = kg ? f32x4{} : fin[2];
#pragma unroll
  for (int e = 0; e < 10; e++) PIN(wr[e]);
  asm volatile("" : "+v"(c[0]), "+v"(c[1]), "+v"(c[2]), "+v"(c[3]), "+v"(c[4]));
  if (wave == 0) poll(ws, nb * FREP + FO + (rb >> 2) * 32 + (lane & 31), tag);
  WAIT_BAR(0);
  const int ob = (int)WS_A3 + ((rb * KS3 + kg * 10) * 4 + rbk) * 1024, o2 = kg ? g * 256 + (lane & 15) * 8 : lane * 16, o3 = o2 + (kg ? 128 : 8);
  bf16x8 of[10];
#pragma unroll
  for (int k = 0; k < 10; k++) {
    if (k < 6) of[k] = LD16(lane * 16, ob + k * 4096);
    else {
      const u32x2 lo = LD8(o2, ob + k * 4096), hi = LD8(o3, ob + k * 4096);
      of[k] = __builtin_bit_cast(bf16x8, u32x4{lo[0], lo[1], hi[0], hi[1]});
    }
  }
  SBAR();
  lds_cptr lw = (lds_cptr)smem + kg * 40960 + lane * 16;
#pragma unroll
  for (int k = 0; k < 10; k++) {
#pragma unroll
    for (int t = 0; t < 5; t++) c[t] = MFMA16(wr[(k & 1) * 5 + t], of[k], c[t]);
    asm volatile("" : "+v"(c[0]), "+v"(c[1]), "+v"(c[2]), "+v"(c[3]), "+v"(c[4]) :: "memory");
    if (k < 8)
#pragma unroll
      for (int t = 0; t < 5; t++) wr[(k & 1) * 5 + t] = lds128(lw + (k * 5 + t) * 1024);
    SBAR();
  }
  LGKM_BAR();
  red[(wave * 3) * 64 + lane] = kg ? c[0] : c[2];
  red[(wave * 3 + 1) * 64 + lane] = kg ? c[1] : c[3];
  if (kg) red[(wave * 3 + 2) * 64 + lane] = c[4];
  LGKM_BAR();
  const f32x4* pr = red + ((wave ^ 4) * 3) * 64 + lane;
  const f32x4 v0 = (kg ? c[2] : c[0]) + pr[0], v1 = (kg ? c[3] : c[1]) + pr[64];
  *(u32x4*)(out + xo + kg * 32 + g * 8) = u32x4{pk2(v0[0], v0[1]), pk2(v0[2], v0[3]), pk2(v1[0], v1[1]), pk2(v1[2], v1[3])};
  if (!kg) { const f32x4 v2 = c[4] + pr[128]; *(u32x2*)(out + xo + 64 + g * 4) = u32x2{pk2(v2[0], v2[1]), pk2(v2[2], v2[3])}; }
}

__global__ __launch_bounds__(512, 4) void fused_block(const u16* __restrict__ x, const u16* __restrict__ wqkv, const u16* __restrict__ wg,
                                                      const u16* __restrict__ wu, const u16* __restrict__ wo, const u16* __restrict__ wd,
                                                      char* __restrict__ ws, u16* __restrict__ out) {
  __shared__ __attribute__((aligned(16))) char smem[K2_LDS];
  const int tid = threadIdx.x;
  const int wave = __builtin_amdgcn_readfirstlane(tid >> 6);
  const int b = blockIdx.x, xcd = b & 7, o = (b >> 3) & 31, cg = xcd >> 1, t = o >> 3, rb = (xcd & 1) * 8 + (o & 7);
  int slot;
  if (b >= 256) {
    const int u = min(tid - 320, 159);
    const char* pf = u < 0 ? (const char*)(x + (rb * 128 + t * 32) * D_) + tid * 128 : (const char*)(wqkv + (o + (u >> 3) * 32) * N1_ + cg * 480) + (u & 7) * 128;
    const unsigned dst = (unsigned)(uintptr_t)smem + 3 * STG + wave * 256;
    asm volatile("s_mov_b32 m0, %1\n\ts_nop 0\n\tglobal_load_lds_dword %0, off" :: "v"(pf), "s"(dst) : "memory", "m0");
    __builtin_amdgcn_s_sleep(96);
    if (wave < 5 || (wave < 7 && !(cg & 1))) {
      const int kr = wave < 5 ? (o & 7) + (tid >> 2) * 8 : o + (min(u, 79) >> 2) * 32;
      pf = (const char*)((tid & 2 ? wu : wg) + kr * F_ + ((cg >> 1) * 9 + (wave < 5 ? (cg & 1) * 5 + t : 4)) * 96) + (tid & 1) * 188;
      asm volatile("s_mov_b32 m0, %1\n\ts_nop 0\n\tglobal_load_lds_dword %0, off" :: "v"(pf), "s"(dst) : "memory", "m0");
    }
  }
  if (b < 256) __builtin_amdgcn_s_setprio(2); else __builtin_amdgcn_s_setprio(1);
  if (b < 256 && t < 3) {
    const int ct = cg * 3 + t;
    if (ct < 8) k1_tile<0>(smem, x, wqkv, wqkv, ws, rb, ct, tid, wave);
    else k1_tile<1>(smem, x, wqkv, wqkv, ws, rb, ct, tid, wave);
    slot = FQ + (ct & 3) * 64 + (ct >> 2) * 16 + rb;
  } else if (b >= 256 || !(cg & 1)) {
    const int ct = (cg >> 1) * 9 + (b < 256 ? 4 : (cg & 1) * 5 + t);
    k1_tile<2>(smem, x, wg, wu, ws, rb, ct, tid, wave);
    slot = FG + rb * 32 + ct;
  } else {
    __builtin_amdgcn_s_sleep(80); __builtin_amdgcn_s_sleep(80);
    retile_role(smem, wo, wd, ws, (cg >> 1) * 16 + rb, tid, wave);
    slot = FR + (cg >> 1) * 16 + rb;
  }
  const unsigned tag = __builtin_amdgcn_readfirstlane(__hip_atomic_load((gu32*)(ws + WS_FLG), RLX)) + 1u;
  publish(ws, slot, tag, tid);
  if (b < 256) __builtin_amdgcn_s_setprio(1); else __builtin_amdgcn_s_setprio(0);
  if (b < 256) {
    attn_role(smem, ws, tag, xcd * 4 + (o >> 3), o & 7, tid, wave);
  } else {
    down_role(smem, x, ws, out, tag, (xcd & 3) * 8 + (o & 7), (xcd >> 2) * 4 + (o >> 3), tid, wave);
    if (b == 256) {
      poll(ws, FG + (tid >> 5) * 32 + min(tid & 31, 17), tag);
      publish(ws, 0, tag, tid);
    }
  }
}

extern "C" void kernel_launch(void* const* d_in, const int* in_sizes, int n_in, void* d_out, int out_size, void* d_ws, size_t ws_size,
                              hipStream_t stream) {
  fused_block<<<512, 512, 0, stream>>>((const u16*)d_in[0], (const u16*)d_in[1], (const u16*)d_in[4], (const u16*)d_in[5], (const u16*)d_in[2],
                                       (const u16*)d_in[6], (char*)d_ws, (u16*)d_out);
}
```

```cpp
#include <hip/hip_runtime.h>
#include <hip/hip_bf16.h>

typedef unsigned short u16;
typedef __attribute__((ext_vector_type(8))) __bf16 bf16x8;
typedef __attribute__((ext_vector_type(2))) __bf16 bf16x2;
typedef __attribute__((ext_vector_type(4))) short s16x4;
typedef __attribute__((ext_vector_type(8))) short s16x8;
typedef __attribute__((ext_vector_type(16))) float f32x16;
typedef __attribute__((ext_vector_type(4))) float f32x4;
typedef __attribute__((ext_vector_type(2))) float f32x2;
typedef __attribute__((ext_vector_type(4))) unsigned u32x4;
typedef __attribute__((ext_vector_type(2))) unsigned u32x2;
#define LAS __attribute__((address_space(3)))
typedef LAS const char* lds_cptr;
typedef __attribute__((address_space(1))) unsigned gu32;

constexpr int S_ = 2048, D_ = 640, F_ = 1728, N1_ = 1920;
constexpr int QT = 1280;
constexpr int VB = 352;
constexpr int KVT = 1024 + 4 * VB;
constexpr int KVP = 2 * KVT + 512;
constexpr int KVH = 32 * KVP;
constexpr int KS3 = 74;
constexpr size_t WS_ST = 0;
constexpr size_t WS_Q = WS_ST + 16384;
constexpr size_t WS_KV = WS_Q + (size_t)32 * 64 * QT;
constexpr size_t WS_A3 = WS_KV + (size_t)32 * KVH;
constexpr size_t WS_W3 = WS_A3 + (size_t)32 * KS3 * 4096;
constexpr size_t WS_FLG = WS_W3 + (size_t)8 * KS3 * 5120;
constexpr int FREP = 1536;
constexpr int FQ = 64, FG = 512, FR = 1024, FO = 1152;

#define MFMA32(a, b, c) __builtin_amdgcn_mfma_f32_32x32x16_bf16(a, b, c, 0, 0, 0)
#define MFMA32X8(a, b, c) __builtin_amdgcn_mfma_f32_32x32x8bf16_1k(a, b, c, 0, 0, 0)
#define MFMA16(a, b, c) __builtin_amdgcn_mfma_f32_16x16x32_bf16(a, b, c, 0, 0, 0)
#define WSR __builtin_amdgcn_make_buffer_rsrc(ws, 0, (int)WS_FLG, 0x00020000)
#define ST16(p, v) __builtin_amdgcn_raw_buffer_store_b128(v, WSR, (int)((p) - ws), 0, 16)
#define ST8(p, v) __builtin_amdgcn_raw_buffer_store_b64(v, WSR, (int)((p) - ws), 0, 16)
#define LD16(vo, so) __builtin_bit_cast(bf16x8, __builtin_amdgcn_raw_buffer_load_b128(WSR, vo, so, 16))
#define LD8(vo, so) __builtin_amdgcn_raw_buffer_load_b64(WSR, vo, so, 16)
#define RLX __ATOMIC_RELAXED, __HIP_MEMORY_SCOPE_AGENT
#define SBAR() __builtin_amdgcn_sched_barrier(0)
#define PIN(x) asm volatile("" : "+v"(x))
#define WAIT_BAR(N) asm volatile("s_waitcnt vmcnt(" #N ") lgkmcnt(0)\n\ts_barrier" ::: "memory")
#define LGKM_BAR() asm volatile("s_waitcnt lgkmcnt(0)\n\ts_barrier" ::: "memory")

__device__ __forceinline__ void glds16(const void* g, unsigned lds_base) {
  asm volatile("s_mov_b32 m0, %1\n\ts_nop 0\n\tglobal_load_lds_dwordx4 %0, off" :: "v"(g), "s"(lds_base) : "memory", "m0");
}
__device__ __forceinline__ void glds16s(const void* base, unsigned off, unsigned lds_base) {
  asm volatile("s_mov_b32 m0, %2\n\ts_nop 0\n\tglobal_load_lds_dwordx4 %0, %1" :: "v"(off), "s"(base), "s"(lds_base) : "memory", "m0");
}
__device__ __forceinline__ void glds16d(const void* g, unsigned lds_base) {
  asm volatile("s_mov_b32 m0, %1\n\ts_nop 0\n\tglobal_load_lds_dwordx4 %0, off sc1" :: "v"(g), "s"(lds_base) : "memory", "m0");
}
__device__ __forceinline__ void publish(char* ws, int slot, unsigned tag, int tid) {
  WAIT_BAR(0);
  if (tid < 8) __hip_atomic_store((gu32*)(ws + WS_FLG) + tid * FREP + slot, tag, RLX);
}
__device__ __forceinline__ void poll(char* ws, int slot, unsigned tag) {
  for (int i = 0; i < (1 << 24); i++) {
    if (__all(__hip_atomic_load((gu32*)(ws + WS_FLG) + slot, RLX) == tag)) break;
    __builtin_amdgcn_s_sleep(4);
  }
}
__device__ __forceinline__ unsigned pk2(float lo, float hi) {
  f32x2 v = {lo, hi};
  return __builtin_bit_cast(unsigned, __builtin_convertvector(v, bf16x2));
}
__device__ __forceinline__ float bfr(float v) { return (float)(__bf16)v; }
__device__ __forceinline__ float bflo(unsigned u) { return __uint_as_float(u << 16); }
__device__ __forceinline__ float bfhi(unsigned u) { return __uint_as_float(u & 0xffff0000u); }
__device__ __forceinline__ bf16x8 lds128(lds_cptr p) { return *(const LAS bf16x8*)p; }
__device__ __forceinline__ s16x4 lds64(lds_cptr p) { return *(const LAS s16x4*)p; }
__device__ __forceinline__ bf16x8 ldstr2(lds_cptr p, int second) {
  s16x4 a = __builtin_amdgcn_ds_read_tr16_b64_v4i16((LAS s16x4*)p);
  s16x4 b = __builtin_amdgcn_ds_read_tr16_b64_v4i16((LAS s16x4*)(p + second));
  return __builtin_bit_cast(bf16x8, __builtin_shufflevector(a, b, 0, 1, 2, 3, 4, 5, 6, 7));
}
__device__ __forceinline__ float xhalf_max(float v) {
  auto r = __builtin_amdgcn_permlane32_swap(__float_as_uint(v), __float_as_uint(v), false, false);
  return fmaxf(__uint_as_float(r[0]), __uint_as_float(r[1]));
}
__device__ __forceinline__ void ln_stats(float sum, float ssq, float& mean_b, float& std_b) {
  float mean = sum * (1.0f / D_);
  float var = fmaxf(ssq * (1.0f / D_) - mean * mean, 0.0f);
  mean_b = bfr(mean);
  std_b = bfr(sqrtf(bfr(bfr(var) + 1e-5f)));
}

constexpr int STG = 12288, X0 = 4 * STG;
#define WAIT_BARN(N) asm volatile("s_waitcnt vmcnt(%0) lgkmcnt(0)\n\ts_barrier" :: "n"(N) : "memory")

template <int MODE>
struct K1Core {
  static constexpr int NB = MODE == 2 ? 6 : 5;
  f32x4 acc[NB][2], cs[2], rs, sq;
  bf16x8 sel;
  const u16 *xs, *wpa, *wpb;
  unsigned xl, wla, wlb;
  int wstep, xo, wo[5], wave;
  unsigned lds0;
  lds_cptr lb;
  static constexpr int ROW = MODE == 2 ? 192 : 320;
  __device__ __forceinline__ void issue_x(int h) {
    const unsigned dst = lds0 + X0 + (h & 2) * 8192 + wave * 2048;
    glds16s(xs + h * 32, xl, dst); glds16s(xs + h * 32 + 8 * D_, xl ^ 64, dst + 1024);
  }
  template <int PAR, int WM> __device__ __forceinline__ void issue_w(int h) {
    const unsigned dst = lds0 + (h & 3) * STG;
    glds16s(wpa + h * wstep, wla, dst + wave * 1024);
    if (MODE == 2 ? (WM >> 1) == PAR : WM == 2 * PAR) glds16s(wpb + h * wstep, wlb, dst + (8 + (wave & 3)) * 1024);
  }
  template <int PAR, int WM> __device__ __forceinline__ void step(int h, bool more) {
    int so = (h & 3) * STG, sx = (h & 2) * 8192;
    asm volatile("" : "+s"(so), "+s"(sx));
    lds_cptr st = lb + so;
    bf16x8 xf[2], wf[NB];
    xf[0] = lds128(lb + sx + (xo ^ (PAR * 64))); xf[1] = lds128(lb + sx + (xo ^ (PAR * 64) ^ 2048));
#pragma unroll
    for (int j = 0; j < NB; j++) wf[j] = ldstr2(st + (MODE == 2 ? wo[j % 3] + (j / 3) * 6144 : wo[j]), 4 * ROW);
    SBAR();
    if (more) { if (!PAR) issue_x(h + 2); issue_w<PAR ^ 1, WM>(h + 3); }
    SBAR();
    rs = MFMA16(sel, xf[0], rs);
    if (MODE != 2) sq = MFMA16(xf[0], xf[0], sq);
#pragma unroll
    for (int j = 0; j < NB; j++) {
#pragma unroll
      for (int a = 0; a < 2; a++) acc[j][a] = MODE == 1 ? MFMA16(xf[a], wf[j], acc[j][a]) : MFMA16(wf[j], xf[a], acc[j][a]);
      if ((j & 3) == WM) cs[j >> 2] = MFMA16(sel, wf[j], cs[j >> 2]);
    }
    SBAR();
#pragma unroll
    for (int j = 0; j < NB; j++) asm volatile("" : "+v"(acc[j][0]), "+v"(acc[j][1]));
    asm volatile("" : "+v"(cs[0]), "+v"(rs));
    if (WM + 4 < NB) PIN(cs[1]);
    if (MODE != 2) PIN(sq);
  }
  template <int WM> __device__ __forceinline__ void run() {
    constexpr int W2 = (MODE == 2 || !(WM & 1)) ? 3 : 2;
    issue_w<0, WM>(0); issue_x(0); issue_w<1, WM>(1); issue_w<0, WM>(2);
#pragma unroll
    for (int j = 0; j < NB; j++) acc[j][0] = acc[j][1] = f32x4{};
    cs[0] = cs[1] = rs = sq = f32x4{};
    for (int h = 0; h < 18; h += 2) {
      WAIT_BARN(W2); step<0, WM>(h, true);
      WAIT_BARN(W2 + 2); step<1, WM>(h + 1, h < 16);
    }
    WAIT_BAR(0); step<0, WM>(18, false); step<1, WM>(19, false);
  }
};

template <int MODE>
__device__ __forceinline__ void k1_tile(char* smem, const u16* __restrict__ x, const u16* __restrict__ w0, const u16* __restrict__ w1,
                                        char* __restrict__ ws, int rb, int ct, int tid, int wave) {
  constexpr int NB = K1Core<MODE>::NB, LD = MODE == 2 ? F_ : N1_;
  const int lane = tid & 63, i = lane & 15, g = lane >> 4, wm = wave >> 1, wn = wave & 1;
  K1Core<MODE> c;
  c.wave = wave; c.lds0 = (unsigned)(uintptr_t)smem; c.lb = (lds_cptr)smem; c.wstep = 32 * LD;
  c.xs = x + (rb * 128 + wave * 16) * D_;
  c.xl = (lane >> 3) * D_ * 2 + (((lane & 7) ^ g) << 4);
  {
    constexpr int CPR = K1Core<MODE>::ROW / 16;
    const int pa = MODE == 2 && wave >= 6 ? wave - 6 : wave, pb = MODE == 2 ? 2 + (wave & 3) : 8 + (wave & 1);
    const int ca = pa * 64 + lane, ra = ca / CPR, cb = pb * 64 + lane, rc = cb / CPR;
    c.wla = ra * LD * 2 + (((ca - ra * CPR) ^ ((ra >> 2) & 2)) << 4);
    c.wlb = rc * LD * 2 + (((cb - rc * CPR) ^ ((rc >> 2) & 2)) << 4);
    c.wpa = (wave >= 6 ? w1 : w0) + ct * (CPR * 8);
    c.wpb = w1 + ct * (CPR * 8);
  }
  c.xo = X0 + wave * 2048 + i * 128 + ((g ^ (i >> 1)) << 4);
  {
    const int q = i >> 2, p = lane & 3;
#pragma unroll
    for (int j = 0; j < 5; j++) {
      const int Q = MODE == 0 ? 5 * p + j + 20 * wn : MODE == 1 ? 4 * j + p + 20 * wn : 4 * (wn ? (j == 2 ? 3 : 4 + j) : j) + p;
      c.wo[j] = (8 * g + q) * K1Core<MODE>::ROW + ((Q ^ (4 * (g & 1))) << 3);
    }
    const unsigned v = i == 0 ? 0x3f803f80u : 0u;
    c.sel = __builtin_bit_cast(bf16x8, u32x4{v, v, v, v});
  }
  if (wm == 0) c.template run<0>();
  else if (wm == 1) c.template run<1>();
  else if (wm == 2) c.template run<2>();
  else c.template run<3>();
  float* sc = (float*)smem;
  if (g == 0) {
    sc[wave * 16 + i] = c.rs[0];
    sc[1792 + wn * 96 + wm * 16 + i] = c.cs[0][0];
    if (wm + 4 < NB) sc[1792 + wn * 96 + (wm + 4) * 16 + i] = c.cs[1][0];
  }
  if (MODE != 2 && g == (i >> 2)) {
    const float s01 = (i & 1) ? c.sq[1] : c.sq[0], s23 = (i & 1) ? c.sq[3] : c.sq[2];
    sc[256 + wave * 16 + i] = (i & 2) ? s23 : s01;
  }
  LGKM_BAR();
  if (tid < 128) {
    if (MODE == 2) {
      sc[1536 + tid] = -bfr(sc[tid] * (1.0f / D_));
    } else {
      float mean_b, std_b;
      ln_stats(sc[tid], sc[256 + tid], mean_b, std_b);
      const float r = 1.0f / std_b;
      sc[1280 + tid] = r; sc[1536 + tid] = -mean_b * r;
      if (ct == 0) __hip_atomic_store((gu32*)(ws + WS_ST) + rb * 128 + tid, __float_as_uint(r * r), RLX);
    }
  }
  LGKM_BAR();
  if (MODE == 0) {
    const bool isq = ct < 4;
    const float scl = isq ? 0.22360679774997896f * 1.4426950408889634f : 1.0f;
    const int head = (ct & 3) * 8 + wn * 4 + g;
    f32x4 cv[5];
#pragma unroll
    for (int j = 0; j < 5; j++) cv[j] = *(const f32x4*)(sc + 1792 + wn * 96 + j * 16 + 4 * g);
#pragma unroll
    for (int a = 0; a < 2; a++) {
      const int blk = a ^ wn, row = wm * 32 + blk * 16 + i, odd = wm & 1;
      const float r = sc[1280 + row] * scl, m = sc[1536 + row] * scl;
      unsigned w[10];
#pragma unroll
      for (int j = 0; j < 5; j++) {
        w[2 * j] = pk2(c.acc[j][a][0] * r + m * cv[j][0], c.acc[j][a][1] * r + m * cv[j][1]);
        w[2 * j + 1] = pk2(c.acc[j][a][2] * r + m * cv[j][2], c.acc[j][a][3] * r + m * cv[j][3]);
      }
      const int rr = isq ? blk * 16 + i : 16 * (i >> 3) + 8 * blk + (i & 7);
      char* tb = isq ? ws + WS_Q + (head * 64 + rb * 4 + wm) * QT : ws + WS_KV + head * KVH + (rb * 2 + (wm >> 1)) * KVP + odd * KVT;
      ST16(tb + rr * 16, (u32x4{w[0], w[1], w[2], w[3]}));
      ST16(tb + 512 + rr * 16, (u32x4{w[4], w[5], w[6], w[7]}));
      if (isq) ST8(tb + 1024 + rr * 8, (u32x2{w[8], w[9]}));
      else *(u32x2*)(smem + 8192 + (((wm >> 1) * 2 + wn) * 4 + g) * 512 + rr * 16 + odd * 8) = u32x2{w[8], w[9]};
    }
    if (!isq) {
      LGKM_BAR();
      const int hl = (wm & 1) * 2 + (lane >> 5);
      ST16(ws + WS_KV + ((ct & 3) * 8 + wn * 4 + hl) * KVH + (rb * 2 + (wm >> 1)) * KVP + 2 * KVT + (lane & 31) * 16,
           *(const u32x4*)(smem + 8192 + (((wm >> 1) * 2 + wn) * 4 + hl) * 512 + (lane & 31) * 16));
    }
  } else if (MODE == 1) {
    {
      const int odd = wm & 1, row = wm * 32 + 4 * g;
      const f32x4 r0 = *(const f32x4*)(sc + 1280 + row), r1 = *(const f32x4*)(sc + 1296 + row);
      const f32x4 m0 = *(const f32x4*)(sc + 1536 + row), m1 = *(const f32x4*)(sc + 1552 + row);
#pragma unroll
      for (int j = 0; j < 5; j++) {
        const int n = 16 * j + i, hl = (n * 52429) >> 20, d = n - hl * 20;
        const float cv = sc[1792 + wn * 96 + n];
        const f32x4 u = (wn ? c.acc[j][1] : c.acc[j][0]) * r0 + m0 * cv, v = (wn ? c.acc[j][0] : c.acc[j][1]) * r1 + m1 * cv;
        char* tb = smem + 8192 + wave * 5632 + hl * 1408 + g * VB + d * 16;
        *(u32x4*)tb = u32x4{pk2(u[0], u[1]), pk2(u[2], u[3]), pk2(v[0], v[1]), pk2(v[2], v[3])};
        if (j > 0 && d >= 18) { const unsigned k = (d == 18) ? 0x3f803f80u : 0u; *(u32x4*)(tb + 32) = u32x4{k, k, k, k}; }
      }
      asm volatile("s_waitcnt lgkmcnt(0)" ::: "memory");
      char* vb = ws + WS_KV + ((ct & 3) * 8 + wn * 4) * KVH + (rb * 2 + (wm >> 1)) * KVP + odd * KVT + 1024;
#pragma unroll
      for (int e = 0; e < 6; e++) {
        const int n = e * 64 + lane, hl = n / 88;
        if (n < 352) ST16(vb + hl * (KVH - 1408) + n * 16, *(const u32x4*)(smem + 8192 + wave * 5632 + n * 16));
      }
    }
  } else {
    f32x4 cv[6];
#pragma unroll
    for (int j = 0; j < 6; j++) cv[j] = *(const f32x4*)(sc + 1792 + wn * 96 + j * 16 + 4 * g);
#pragma unroll
    for (int a = 0; a < 2; a++) {
      const int sub = (wm & 1) * 2 + (a ^ wn);
      const float m = sc[1536 + wm * 32 + (a ^ wn) * 16 + i];
      unsigned w[6];
#pragma unroll
      for (int j = 0; j < 3; j++)
#pragma unroll
        for (int e = 0; e < 2; e++) {
          const unsigned gp = pk2(c.acc[j][a][2 * e] + m * cv[j][2 * e], c.acc[j][a][2 * e + 1] + m * cv[j][2 * e + 1]);
          const unsigned up = pk2(c.acc[j + 3][a][2 * e] + m * cv[j + 3][2 * e], c.acc[j + 3][a][2 * e + 1] + m * cv[j + 3][2 * e + 1]);
          w[2 * j + e] = pk2(bflo(gp) * bflo(up), bfhi(gp) * bfhi(up));
        }
      char* ab = ws + WS_A3 + (((rb * 2 + (wm >> 1)) * KS3 + 20 + ct * 3) * 4 + sub) * 1024;
      ST16(ab + wn * 8192 + lane * 16, (u32x4{w[0], w[1], w[2], w[3]}));
      ST8(ab + 4096 + wn * 512 + (g >> 1) * 256 + i * 16 + (g & 1) * 8, (u32x2{w[4], w[5]}));
    }
  }
}

__device__ __forceinline__ void retile_role(char* smem, const u16* __restrict__ wo, const u16* __restrict__ wd, char* __restrict__ ws,
                                            int idx, int tid, int wave) {
  const int lane = tid & 63, g = lane >> 4, q = (lane & 15) >> 2, p = lane & 3;
  const unsigned lds0 = (unsigned)(uintptr_t)smem;
  for (int k0 = idx; k0 < KS3; k0 += 64) {
    WAIT_BAR(0);
    for (int u = 0; u < 2; u++) {
      const int ks = k0 + 32 * u;
      if (ks < KS3) {
        const int orow = ks < 16 ? (2 * ks + (wave >> 2)) * 20 + (wave & 2) * 2 + (wave & 1) * 8 : (8 * (ks - 16) + wave) * 20 + 16;
        const char* src = (const char*)(ks < 20 ? wo + (size_t)orow * D_ : wd + (size_t)((ks - 20) * 32 + wave * 4) * D_) + lane * 16;
#pragma unroll
        for (int e = 0; e < 5; e++) glds16(src + e * 1024, lds0 + u * 40960 + wave * 5120 + e * 1024);
      }
    }
    WAIT_BAR(0);
    for (int u = 0; u < 2; u++) {
      const int ks = k0 + 32 * u;
      if (ks < KS3) {
        const bool nat = ks < 20 || ks % 3 == 0;
        const int row = nat ? 8 * g + q : 4 * g + q, sec = (nat ? 4 : 16) * 1280;
        lds_cptr b = (lds_cptr)smem + u * 40960 + row * 1280 + wave * 160;
#pragma unroll
        for (int t = 0; t < 5; t++)
          ST16(ws + WS_W3 + ((wave * KS3 + ks) * 5 + t) * 1024 + lane * 16, __builtin_bit_cast(u32x4, ldstr2(b + (t < 4 ? (t >> 1) * 64 + p * 16 + (t & 1) * 8 : 128 + p * 8), sec)));
      }
    }
  }
}

constexpr int AT_BUF = 4 * KVP;
constexpr int K2_LDS = 81920;

#define TOFF(t) (((t) >> 1) * KVP + ((t) & 1) * KVT)
struct KFrag { bf16x8 k1; s16x4 k2; };

__device__ __forceinline__ f32x16 qk_tile(const KFrag& k, bf16x8 q1, s16x4 q2) {
  f32x16 z = {};
  f32x16 s = MFMA32(k.k1, q1, z);
  return MFMA32X8(k.k2, q2, s);
}
__device__ __forceinline__ bf16x8 exp_pack(const f32x16& s, int m) {
  u32x4 w;
#pragma unroll
  for (int i = 0; i < 4; i++) w[i] = pk2(__builtin_amdgcn_exp2f(s[8 * m + 2 * i]), __builtin_amdgcn_exp2f(s[8 * m + 2 * i + 1]));
  return __builtin_bit_cast(bf16x8, w);
}
__device__ __forceinline__ float max16(const f32x16& s) {
  float a = fmaxf(s[0], s[1]);
#pragma unroll
  for (int i = 2; i < 16; i += 2) a = fmaxf(fmaxf(a, s[i]), s[i + 1]);
  return a;
}

#define AT_GROUP(MF, ACC, A, B, P, PJ, SJ) do { unsigned pa_, pb_; SBAR();                                                        \
    asm volatile(MF "\n\tv_cvt_pk_bf16_f32 %1, %3, %4\n\tv_cvt_pk_bf16_f32 %2, %5, %6\n\tv_exp_f32 %3, %9\n\tv_exp_f32 %4, %10\n\t"    \
                 "v_exp_f32 %5, %11\n\tv_exp_f32 %6, %12"                                                                          \
                 : ACC, "=&v"(pa_), "=&v"(pb_), "+v"(e[0]), "+v"(e[1]), "+v"(e[2]), "+v"(e[3])                                     \
                 : "v"(A), "v"(B), "v"(sa[SJ]), "v"(sa[SJ + 1]), "v"(sa[SJ + 2]), "v"(sa[SJ + 3]));                                \
    P[PJ] = pa_; P[PJ + 1] = pb_; SBAR(); } while (0)

__device__ __forceinline__ void attn_role(char* smem, char* __restrict__ ws, unsigned tag, int head, int qb, int tid, int wave) {
  const int lane = tid & 63, r = lane & 31, hh = lane >> 5;
  const unsigned lds0 = (unsigned)(uintptr_t)smem;
  const char* kvsrc = ws + WS_KV + (size_t)head * KVH + wave * 1024 + lane * 16;
  auto dma = [&](int c, int buf) {
    const char* src = kvsrc + (size_t)c * AT_BUF;
    const unsigned dst = lds0 + buf + wave * 1024;
    glds16d(src, dst);
    glds16d(src + 8192, dst + 8192);
    if (wave < 5) glds16d(src + 16384, dst + 16384);
  };
  if (wave == 0) poll(ws, qb * FREP + FQ + (head >> 3) * 64 + min(lane, 47), tag);
  LGKM_BAR();
  dma(0, 0);
  const int qo = (int)WS_Q + (head * 64 + qb * 8 + wave) * QT;
  const bf16x8 q1 = LD16(lane * 16, qo);
  u32x2 q2u = LD8(1024 + r * 8, qo);
  if (hh) q2u = u32x2{0u, 0u};
  const int k1o = lane * 16, k2o = hh ? 1024 + 320 : 2 * KVT + r * 16, vo = 1024 + hh * VB + min(r, 21) * 16;
  lds_cptr lb = (lds_cptr)smem;
  LAS int* flag = (LAS int*)(lb + 3 * AT_BUF + 512);
  auto ldk = [&](int t) { KFrag k; k.k1 = lds128(lb + TOFF(t) + k1o); k.k2 = lds64(lb + (t >> 1) * KVP + (t & 1) * 8 + k2o); return k; };
  auto set_ref = [&](float m) {
    const float m0 = bfr(m), m1 = bfr(m - m0), m2 = bfr(m - m0 - m1);
    if (hh) q2u = u32x2{pk2(-m0, -m1), pk2(-m2, 0.f)};
  };
  WAIT_BAR(0);
  dma(1, AT_BUF);
  {
    KFrag k = ldk(0);
    set_ref(xhalf_max(max16(qk_tile(k, q1, __builtin_bit_cast(s16x4, q2u)))));
  }
  f32x16 o;
  for (int attempt = 0;; attempt++) {
    const s16x4 q2 = __builtin_bit_cast(s16x4, q2u);
    o = f32x16{};
    f32x16 sa;
    { KFrag k = ldk(0); sa = qk_tile(k, q1, q2); }
    bf16x8 k1 = lds128(lb + KVT + k1o), v1 = {}, v0;
    s16x8 k2p = __builtin_bit_cast(s16x8, lds128(lb + k2o));
    v0 = lds128(lb + vo);
    u32x4 p0 = {0u, 0u, 0u, 0u}, p1 = {0u, 0u, 0u, 0u};
    float e[4] = {0.f, 0.f, 0.f, 0.f};
    int cur = 0, nxt = AT_BUF;
    asm volatile("s_nop 15\n\ts_nop 7" : "+v"(sa), "+v"(o), "+v"(p1), "+v"(v1));
    for (int c = 0; c < 8; c++) {
      lds_cptr ck1 = lb + cur + k1o, ck2 = lb + cur + k2o, cv = lb + cur + vo;
      lds_cptr nk1 = lb + nxt + k1o, nk2 = lb + nxt + k2o, nv = lb + nxt + vo;
      const int nn = (nxt == 2 * AT_BUF) ? 0 : nxt + AT_BUF;
#pragma unroll
      for (int t = 0; t < 8; t++) {
        if (t == 6) asm volatile("s_waitcnt vmcnt(0)\n\ts_barrier" ::: "memory");
        const int pc = (t + 2) & 7;
        const bool ld = pc < 3 && (pc < 2 ? c < 6 : (c > 0 && c < 7 && wave < 5));
        const char* src = kvsrc + (size_t)(t == 0 ? c + 1 : c + 2) * AT_BUF + pc * 8192;
        const unsigned dst = lds0 + (t == 0 ? nxt : nn) + wave * 1024 + pc * 8192;
        if (ld && wave < 4) glds16d(src, dst);
        f32x16 sb;
        const s16x4 k2 = (t & 1) ? __builtin_shufflevector(k2p, k2p, 0, 1, 2, 3) : __builtin_shufflevector(k2p, k2p, 4, 5, 6, 7);
        AT_GROUP("v_mfma_f32_32x32x16_bf16 %0, %7, %8, 0", "=&v"(sb), k1, q1, p1, 2, 0);
        k1 = lds128((t < 6 ? ck1 : nk1) + TOFF((t + 2) & 7));
        AT_GROUP("v_mfma_f32_32x32x16_bf16 %0, %7, %8, %0", "+v"(o), v1, p1, p0, 0, 4);
        v1 = lds128(cv + TOFF(t) + 2 * VB);
        if (ld && wave >= 4) glds16d(src, dst);
        AT_GROUP("v_mfma_f32_32x32x8_bf16 %0, %7, %8, %0", "+v"(sb), k2, q2, p0, 2, 8);
        if (!(t & 1)) k2p = __builtin_bit_cast(s16x8, lds128((t < 6 ? ck2 : nk2) + TOFF((t + 2) & 7)));
        AT_GROUP("v_mfma_f32_32x32x16_bf16 %0, %7, %8, %0", "+v"(o), v0, p0, p1, 0, 12);
        v0 = lds128((t < 7 ? cv : nv) + TOFF((t + 1) & 7));
        sa = sb;
      }
      cur = nxt; nxt = nn;
    }
    asm volatile("s_nop 7" : "+v"(o), "+v"(e[3]));
    p1[2] = pk2(e[0], e[1]); p1[3] = pk2(e[2], e[3]);
    o = MFMA32(v1, __builtin_bit_cast(bf16x8, p1), o);
    if (attempt == 1) break;
    bool ok = true;
#pragma unroll
    for (int i = 0; i < 12; i++) ok = ok && (fabsf(o[i]) < 1e30f || (hh && i > 8));
    LGKM_BAR();
    if (tid == 0) *flag = 0;
    LGKM_BAR();
    if (!ok) *flag = 1;
    LGKM_BAR();
    if (*flag == 0) break;
    if (hh) q2u = u32x2{0u, 0u};
    const s16x4 qz = __builtin_bit_cast(s16x4, q2u);
    float mx = -INFINITY;
    for (int c = 0; c < 8; c++) {
      LGKM_BAR();
      dma(c, 0);
      WAIT_BAR(0);
      for (int t = 0; t < 8; t++) { KFrag k = ldk(t); mx = fmaxf(mx, max16(qk_tile(k, q1, qz))); }
    }
    set_ref(xhalf_max(mx));
    LGKM_BAR();
    dma(0, 0); dma(1, AT_BUF);
    WAIT_BAR(0);
  }
  auto sw = __builtin_amdgcn_permlane32_swap(__float_as_uint(o[8]), __float_as_uint(o[8]), false, false);
  const float inv = 1.0f / __uint_as_float(sw[1]);
  const int s = qb * 256 + wave * 32 + r;
  char* ab = ws + WS_A3 + ((s >> 6) * KS3 * 4 + ((s >> 4) & 3)) * 1024;
  ST16(ab + (head >> 1) * 4096 + ((head & 1) * 2 + hh) * 256 + (s & 15) * 16,
       (u32x4{pk2(o[0] * inv, o[1] * inv), pk2(o[2] * inv, o[3] * inv), pk2(o[4] * inv, o[5] * inv), pk2(o[6] * inv, o[7] * inv)}));
  if (!hh) ST8(ab + (16 + (head >> 3)) * 4096 + (head & 7) * 128 + (s & 15) * 8, (u32x2{pk2(o[8] * inv, o[9] * inv), pk2(o[10] * inv, o[11] * inv)}));
  publish(ws, FO + qb * 32 + head, tag, tid);
}

__device__ __forceinline__ void down_role(char* smem, const u16* __restrict__ x, char* __restrict__ ws, u16* __restrict__ out, unsigned tag, int rb, int nb, int tid, int wave) {
  const int lane = tid & 63;
  if (wave == 0) poll(ws, nb * FREP + (lane < 18 ? FG + (rb >> 1) * 32 + lane : lane < 32 ? FQ + (rb >> 1) : FR - 32 + lane), tag);
  LGKM_BAR();
  int ap = (int)WS_A3 + (rb * KS3 + 20 + wave) * 4096, wp = (int)WS_W3 + (nb * KS3 + 20 + wave) * 5120;
  bf16x8 a[4], w[5];
#pragma unroll
  for (int t = 0; t < 5; t++) w[t] = LD16(lane * 16 + (t & 3) * 1024, wp + (t >> 2) * 4096);
#pragma unroll
  for (int i = 0; i < 4; i++) a[i] = LD16(lane * 16 + i * 1024, ap);
  f32x4 acc[20] = {};
  const int n = wave < 6 ? 7 : 6;
  for (int it = 1; it <= n; it++) {
    if (it < n) { ap += 8 * 4096; wp += 8 * 5120; }
#pragma unroll
    for (int i = 0; i < 4; i++) {
#pragma unroll
      for (int t = 0; t < 5; t++) acc[t * 4 + i] = MFMA16(w[t], a[i], acc[t * 4 + i]);
      SBAR();
      a[i] = LD16(lane * 16 + i * 1024, ap);
    }
#pragma unroll
    for (int t = 0; t < 5; t++) w[t] = LD16(lane * 16 + (t & 3) * 1024, wp + (t >> 2) * 4096);
  }
  f32x4* red = (f32x4*)smem;
  const int rbk = wave & 3, kg = wave >> 2, g = lane >> 4, row = rb * 64 + rbk * 16 + (lane & 15);
  const size_t xo = (size_t)row * D_ + nb * 80;
  const u32x4 xv = *(const u32x4*)(x + xo + kg * 32 + g * 8);
  const u32x2 xw = *(const u32x2*)(x + xo + 64 + g * 4);
  const float r2 = __uint_as_float(__hip_atomic_load((gu32*)(ws + WS_ST) + row, RLX));
  f32x4 fin[3];
#pragma unroll
  for (int rd = 0; rd < 3; rd++) {
    if (rd) LGKM_BAR();
#pragma unroll
    for (int u = 0; u < (rd < 2 ? 8 : 4); u++) red[(wave * 8 + u) * 64 + lane] = acc[(rd < 2 ? 2 * (u >> 2) + rd : 4) * 4 + (u & 3)];
    LGKM_BAR();
    f32x4 v = red[(rd < 2 ? wave : rbk) * 64 + lane];
#pragma unroll
    for (int k = 1; k < 8; k++) v += red[(k * 8 + (rd < 2 ? wave : rbk)) * 64 + lane];
    const unsigned x0 = rd < 2 ? xv[2 * rd] : xw[0], x1 = rd < 2 ? xv[2 * rd + 1] : xw[1];
    fin[rd] = v * r2 + f32x4{bflo(x0), bfhi(x0), bflo(x1), bfhi(x1)};
    PIN(fin[rd]);
  }
  LGKM_BAR();
  const char* wo = ws + WS_W3 + ((size_t)nb * KS3 + kg * 10) * 5120 + lane * 16;
  const unsigned lds0 = (unsigned)(uintptr_t)smem;
#pragma unroll
  for (int e = 0; e < 10; e++) glds16d(wo + (10 + rbk * 10 + e) * 1024, lds0 + (wave * 10 + e) * 1024);
  bf16x8 wr[10];
#pragma unroll
  for (int e = 0; e < 10; e++) wr[e] = LD16(lane * 16 + (e & 3) * 1024, (int)WS_W3 + (nb * KS3 + kg * 10) * 5120 + (e >> 2) * 4096);
  f32x4 c[5];
  c[0] = kg ? f32x4{} : fin[0]; c[1] = kg ? f32x4{} : fin[1]; c[2] = kg ? fin[0] : f32x4{}; c[3] = kg ? fin[1] : f32x4{}; c[4] = kg ? f32x4{} : fin[2];
#pragma unroll
  for (int e = 0; e < 10; e++) PIN(wr[e]);
  asm volatile("" : "+v"(c[0]), "+v"(c[1]), "+v"(c[2]), "+v"(c[3]), "+v"(c[4]));
  if (rb == 0 && nb == 0) poll(ws, FG + (tid >> 5) * 32 + min(tid & 31, 17), tag);
  if (wave == 0) poll(ws, nb * FREP + FO + (rb >> 2) * 32 + (lane & 31), tag);
  WAIT_BAR(0);
  if (rb == 0 && nb == 0 && tid == 0) __hip_atomic_store((gu32*)(ws + WS_FLG), tag, RLX);
  const int ob = (int)WS_A3 + ((rb * KS3 + kg * 10) * 4 + rbk) * 1024, o2 = kg ? g * 256 + (lane & 15) * 8 : lane * 16, o3 = o2 + (kg ? 128 : 8);
  bf16x8 of[10];
#pragma unroll
  for (int k = 0; k < 10; k++) {
    if (k < 6) of[k] = LD16(lane * 16, ob + k * 4096);
    else {
      const u32x2 lo = LD8(o2, ob + k * 4096), hi = LD8(o3, ob + k * 4096);
      of[k] = __builtin_bit_cast(bf16x8, u32x4{lo[0], lo[1], hi[0], hi[1]});
    }
  }
  SBAR();
  lds_cptr lw = (lds_cptr)smem + kg * 40960 + lane * 16;
#pragma unroll
  for (int k = 0; k < 10; k++) {
#pragma unroll
    for (int t = 0; t < 5; t++) c[t] = MFMA16(wr[(k & 1) * 5 + t], of[k], c[t]);
    asm volatile("" : "+v"(c[0]), "+v"(c[1]), "+v"(c[2]), "+v"(c[3]), "+v"(c[4]) :: "memory");
    if (k < 8)
#pragma unroll
      for (int t = 0; t < 5; t++) wr[(k & 1) * 5 + t] = lds128(lw + (k * 5 + t) * 1024);
    SBAR();
  }
  LGKM_BAR();
  red[(wave * 3) * 64 + lane] = kg ? c[0] : c[2];
  red[(wave * 3 + 1) * 64 + lane] = kg ? c[1] : c[3];
  if (kg) red[(wave * 3 + 2) * 64 + lane] = c[4];
  LGKM_BAR();
  const f32x4* pr = red + ((wave ^ 4) * 3) * 64 + lane;
  const f32x4 v0 = (kg ? c[2] : c[0]) + pr[0], v1 = (kg ? c[3] : c[1]) + pr[64];
  *(u32x4*)(out + xo + kg * 32 + g * 8) = u32x4{pk2(v0[0], v0[1]), pk2(v0[2], v0[3]), pk2(v1[0], v1[1]), pk2(v1[2], v1[3])};
  if (!kg) { const f32x4 v2 = c[4] + pr[128]; *(u32x2*)(out + xo + 64 + g * 4) = u32x2{pk2(v2[0], v2[1]), pk2(v2[2], v2[3])}; }
}

__global__ __launch_bounds__(512, 4) void fused_block(const u16* __restrict__ x, const u16* __restrict__ wqkv, const u16* __restrict__ wg,
                                                      const u16* __restrict__ wu, const u16* __restrict__ wo, const u16* __restrict__ wd,
                                                      char* __restrict__ ws, u16* __restrict__ out) {
  __shared__ __attribute__((aligned(16))) char smem[K2_LDS];
  const int tid = threadIdx.x;
  const int wave = __builtin_amdgcn_readfirstlane(tid >> 6);
  const int b = blockIdx.x, xcd = b & 7, o = (b >> 3) & 31, cg = xcd >> 1, t = o >> 3, rb = (xcd & 1) * 8 + (o & 7);
  int slot;
  if (b >= 256) {
    const int u = min(tid - 320, 159);
    const char* pf = u < 0 ? (const char*)(x + (rb * 128 + t * 32) * D_) + tid * 128 : (const char*)(wqkv + (o + (u >> 3) * 32) * N1_ + cg * 480) + (u & 7) * 128;
    const unsigned dst = (unsigned)(uintptr_t)smem + 3 * STG + wave * 256;
    if (o == 0 && wave < 6) {
      const char* pc;
      asm volatile("s_getpc_b64 %0" : "=s"(pc));
      asm volatile("s_mov_b32 m0, %1\n\ts_nop 0\n\tglobal_load_lds_dword %0, off" :: "v"(pc + tid * 128), "s"(dst) : "memory", "m0");
    }
    if (o == 1 && wave < 6) {
      const char* pg = tid < 336 ? ws + tid * 65536 : (const char*)out + min(tid - 336, 39) * 65536;
      asm volatile("s_mov_b32 m0, %1\n\ts_nop 0\n\tglobal_load_lds_dword %0, off sc1" :: "v"(pg), "s"(dst) : "memory", "m0");
    }
    asm volatile("s_mov_b32 m0, %1\n\ts_nop 0\n\tglobal_load_lds_dword %0, off" :: "v"(pf), "s"(dst) : "memory", "m0");
    __builtin_amdgcn_s_sleep(96);
    if (wave < 5 || (wave < 7 && !(cg & 1))) {
      const int kr = wave < 5 ? (o & 7) + (tid >> 2) * 8 : o + (min(u, 79) >> 2) * 32;
      pf = (const char*)((tid & 2 ? wu : wg) + kr * F_ + ((cg >> 1) * 9 + (wave < 5 ? (cg & 1) * 5 + t : 4)) * 96) + (tid & 1) * 188;
      asm volatile("s_mov_b32 m0, %1\n\ts_nop 0\n\tglobal_load_lds_dword %0, off" :: "v"(pf), "s"(dst) : "memory", "m0");
    }
  }
  if (b < 256) __builtin_amdgcn_s_setprio(2); else __builtin_amdgcn_s_setprio(1);
  if (b < 256 && t < 3) {
    const int ct = cg * 3 + t;
    if (ct < 8) k1_tile<0>(smem, x, wqkv, wqkv, ws, rb, ct, tid, wave);
    else k1_tile<1>(smem, x, wqkv, wqkv, ws, rb, ct, tid, wave);
    slot = FQ + (ct & 3) * 64 + (ct >> 2) * 16 + rb;
  } else if (b >= 256 || !(cg & 1)) {
    const int ct = (cg >> 1) * 9 + (b < 256 ? 4 : (cg & 1) * 5 + t);
    k1_tile<2>(smem, x, wg, wu, ws, rb, ct, tid, wave);
    slot = FG + rb * 32 + ct;
  } else {
    __builtin_amdgcn_s_sleep(80); __builtin_amdgcn_s_sleep(80);
    retile_role(smem, wo, wd, ws, (cg >> 1) * 16 + rb, tid, wave);
    slot = FR + (cg >> 1) * 16 + rb;
  }
  const unsigned tag = __builtin_amdgcn_readfirstlane(__hip_atomic_load((gu32*)(ws + WS_FLG), RLX)) + 1u;
  publish(ws, slot, tag, tid);
  if (b < 256) __builtin_amdgcn_s_setprio(1); else __builtin_amdgcn_s_setprio(0);
  if (b < 256) {
    attn_role(smem, ws, tag, xcd * 4 + (o >> 3), o & 7, tid, wave);
  } else {
    down_role(smem, x, ws, out, tag, (xcd & 3) * 8 + (o & 7), (xcd >> 2) * 4 + (o >> 3), tid, wave);
  }
}

extern "C" void kernel_launch(void* const* d_in, const int* in_sizes, int n_in, void* d_out, int out_size, void* d_ws, size_t ws_size,
                              hipStream_t stream) {
  fused_block<<<512, 512, 0, stream>>>((const u16*)d_in[0], (const u16*)d_in[1], (const u16*)d_in[4], (const u16*)d_in[5], (const u16*)d_in[2],
                                       (const u16*)d_in[6], (char*)d_ws, (u16*)d_out);
}
```

```cpp
#include <hip/hip_runtime.h>
#include <hip/hip_bf16.h>

typedef unsigned short u16;
typedef __attribute__((ext_vector_type(8))) __bf16 bf16x8;
typedef __attribute__((ext_vector_type(2))) __bf16 bf16x2;
typedef __attribute__((ext_vector_type(4))) short s16x4;
typedef __attribute__((ext_vector_type(8))) short s16x8;
typedef __attribute__((ext_vector_type(16))) float f32x16;
typedef __attribute__((ext_vector_type(4))) float f32x4;
typedef __attribute__((ext_vector_type(2))) float f32x2;
typedef __attribute__((ext_vector_type(4))) unsigned u32x4;
typedef __attribute__((ext_vector_type(2))) unsigned u32x2;
#define LAS __attribute__((address_space(3)))
typedef LAS const char* lds_cptr;
typedef __attribute__((address_space(1))) unsigned gu32;

constexpr int S_ = 2048, D_ = 640, F_ = 1728, N1_ = 1920;
constexpr int QT = 1280;
constexpr int VB = 352;
constexpr int KVT = 1024 + 4 * VB;
constexpr int KVP = 2 * KVT + 512;
constexpr int KVH = 32 * KVP;
constexpr int KS3 = 74;
constexpr size_t WS_ST = 0;
constexpr size_t WS_Q = WS_ST + 16384;
constexpr size_t WS_KV = WS_Q + (size_t)32 * 64 * QT;
constexpr size_t WS_A3 = WS_KV + (size_t)32 * KVH;
constexpr size_t WS_W3 = WS_A3 + (size_t)32 * KS3 * 4096;
constexpr size_t WS_FLG = WS_W3 + (size_t)8 * KS3 * 5120;
constexpr int FREP = 1536;
constexpr int FQ = 64, FG = 512, FR = 1024, FO = 1152;

#define MFMA32(a, b, c) __builtin_amdgcn_mfma_f32_32x32x16_bf16(a, b, c, 0, 0, 0)
#define MFMA32X8(a, b, c) __builtin_amdgcn_mfma_f32_32x32x8bf16_1k(a, b, c, 0, 0, 0)
#define MFMA16(a, b, c) __builtin_amdgcn_mfma_f32_16x16x32_bf16(a, b, c, 0, 0, 0)
#define WSR __builtin_amdgcn_make_buffer_rsrc(ws, 0, (int)WS_FLG, 0x00020000)
#define ST16(p, v) __builtin_amdgcn_raw_buffer_store_b128(v, WSR, (int)((p) - ws), 0, 16)
#define ST8(p, v) __builtin_amdgcn_raw_buffer_store_b64(v, WSR, (int)((p) - ws), 0, 16)
#define LD16(vo, so) __builtin_bit_cast(bf16x8, __builtin_amdgcn_raw_buffer_load_b128(WSR, vo, so, 16))
#define LD8(vo, so) __builtin_amdgcn_raw_buffer_load_b64(WSR, vo, so, 16)
#define RLX __ATOMIC_RELAXED, __HIP_MEMORY_SCOPE_AGENT
#define SBAR() __builtin_amdgcn_sched_barrier(0)
#define PIN(x) asm volatile("" : "+v"(x))
#define WAIT_BAR(N) asm volatile("s_waitcnt vmcnt(" #N ") lgkmcnt(0)\n\ts_barrier" ::: "memory")
#define LGKM_BAR() asm volatile("s_waitcnt lgkmcnt(0)\n\ts_barrier" ::: "memory")

__device__ __forceinline__ void glds16(const void* g, unsigned lds_base) {
  asm volatile("s_mov_b32 m0, %1\n\ts_nop 0\n\tglobal_load_lds_dwordx4 %0, off" :: "v"(g), "s"(lds_base) : "memory", "m0");
}
__device__ __forceinline__ void glds16s(const void* base, unsigned off, unsigned lds_base) {
  asm volatile("s_mov_b32 m0, %2\n\ts_nop 0\n\tglobal_load_lds_dwordx4 %0, %1" :: "v"(off), "s"(base), "s"(lds_base) : "memory", "m0");
}
__device__ __forceinline__ void glds16d(const void* g, unsigned lds_base) {
  asm volatile("s_mov_b32 m0, %1\n\ts_nop 0\n\tglobal_load_lds_dwordx4 %0, off sc1" :: "v"(g), "s"(lds_base) : "memory", "m0");
}
__device__ __forceinline__ void publish(char* ws, int slot, unsigned tag, int tid) {
  WAIT_BAR(0);
  if (tid < 8) __hip_atomic_store((gu32*)(ws + WS_FLG) + tid * FREP + slot, tag, RLX);
}
__device__ __forceinline__ void poll(char* ws, int slot, unsigned tag) {
  for (int i = 0; i < (1 << 24); i++) {
    if (__all(__hip_atomic_load((gu32*)(ws + WS_FLG) + slot, RLX) == tag)) break;
    __builtin_amdgcn_s_sleep(4);
  }
}
__device__ __forceinline__ unsigned pk2(float lo, float hi) {
  f32x2 v = {lo, hi};
  return __builtin_bit_cast(unsigned, __builtin_convertvector(v, bf16x2));
}
__device__ __forceinline__ float bfr(float v) { return (float)(__bf16)v; }
__device__ __forceinline__ float bflo(unsigned u) { return __uint_as_float(u << 16); }
__device__ __forceinline__ float bfhi(unsigned u) { return __uint_as_float(u & 0xffff0000u); }
__device__ __forceinline__ bf16x8 lds128(lds_cptr p) { return *(const LAS bf16x8*)p; }
__device__ __forceinline__ s16x4 lds64(lds_cptr p) { return *(const LAS s16x4*)p; }
__device__ __forceinline__ bf16x8 ldstr2(lds_cptr p, int second) {
  s16x4 a = __builtin_amdgcn_ds_read_tr16_b64_v4i16((LAS s16x4*)p);
  s16x4 b = __builtin_amdgcn_ds_read_tr16_b64_v4i16((LAS s16x4*)(p + second));
  return __builtin_bit_cast(bf16x8, __builtin_shufflevector(a, b, 0, 1, 2, 3, 4, 5, 6, 7));
}
__device__ __forceinline__ float xhalf_max(float v) {
  auto r = __builtin_amdgcn_permlane32_swap(__float_as_uint(v), __float_as_uint(v), false, false);
  return fmaxf(__uint_as_float(r[0]), __uint_as_float(r[1]));
}
__device__ __forceinline__ void ln_stats(float sum, float ssq, float& mean_b, float& std_b) {
  float mean = sum * (1.0f / D_);
  float var = fmaxf(ssq * (1.0f / D_) - mean * mean, 0.0f);
  mean_b = bfr(mean);
  std_b = bfr(sqrtf(bfr(bfr(var) + 1e-5f)));
}

constexpr int STG = 12288, X0 = 4 * STG;
#define WAIT_BARN(N) asm volatile("s_waitcnt vmcnt(%0) lgkmcnt(0)\n\ts_barrier" :: "n"(N) : "memory")

template <int MODE>
struct K1Core {
  static constexpr int NB = MODE == 2 ? 6 : 5;
  f32x4 acc[NB][2], cs[2], rs, sq;
  bf16x8 sel;
  const u16 *xs, *wpa, *wpb;
  unsigned xl, wla, wlb;
  int wstep, xo, wo[5], wave;
  unsigned lds0;
  lds_cptr lb;
  static constexpr int ROW = MODE == 2 ? 192 : 320;
  __device__ __forceinline__ void issue_x(int h) {
    const unsigned dst = lds0 + X0 + (h & 2) * 8192 + wave * 2048;
    glds16s(xs + h * 32, xl, dst); glds16s(xs + h * 32 + 8 * D_, xl ^ 64, dst + 1024);
  }
  template <int PAR, int WM> __device__ __forceinline__ void issue_w(int h) {
    const unsigned dst = lds0 + (h & 3) * STG;
    glds16s(wpa + h * wstep, wla, dst + wave * 1024);
    if (MODE == 2 ? (WM >> 1) == PAR : WM == 2 * PAR) glds16s(wpb + h * wstep, wlb, dst + (8 + (wave & 3)) * 1024);
  }
  template <int PAR, int WM> __device__ __forceinline__ void step(int h, bool more) {
    int so = (h & 3) * STG, sx = (h & 2) * 8192;
    asm volatile("" : "+s"(so), "+s"(sx));
    lds_cptr st = lb + so;
    bf16x8 xf[2], wf[NB];
    xf[0] = lds128(lb + sx + (xo ^ (PAR * 64))); xf[1] = lds128(lb + sx + (xo ^ (PAR * 64) ^ 2048));
#pragma unroll
    for (int j = 0; j < NB; j++) wf[j] = ldstr2(st + (MODE == 2 ? wo[j % 3] + (j / 3) * 6144 : wo[j]), 4 * ROW);
    SBAR();
    if (more) { if (!PAR) issue_x(h + 2); issue_w<PAR ^ 1, WM>(h + 3); }
    SBAR();
    rs = MFMA16(sel, xf[0], rs);
    if (MODE != 2) sq = MFMA16(xf[0], xf[0], sq);
#pragma unroll
    for (int j = 0; j < NB; j++) {
#pragma unroll
      for (int a = 0; a < 2; a++) acc[j][a] = MODE == 1 ? MFMA16(xf[a], wf[j], acc[j][a]) : MFMA16(wf[j], xf[a], acc[j][a]);
      if ((j & 3) == WM) cs[j >> 2] = MFMA16(sel, wf[j], cs[j >> 2]);
    }
    SBAR();
#pragma unroll
    for (int j = 0; j < NB; j++) asm volatile("" : "+v"(acc[j][0]), "+v"(acc[j][1]));
    asm volatile("" : "+v"(cs[0]), "+v"(rs));
    if (WM + 4 < NB) PIN(cs[1]);
    if (MODE != 2) PIN(sq);
  }
  template <int WM> __device__ __forceinline__ void run() {
    constexpr int W2 = (MODE == 2 || !(WM & 1)) ? 3 : 2;
    issue_w<0, WM>(0); issue_x(0); issue_w<1, WM>(1); issue_w<0, WM>(2);
#pragma unroll
    for (int j = 0; j < NB; j++) acc[j][0] = acc[j][1] = f32x4{};
    cs[0] = cs[1] = rs = sq = f32x4{};
    for (int h = 0; h < 18; h += 2) {
      WAIT_BARN(W2); step<0, WM>(h, true);
      WAIT_BARN(W2 + 2); step<1, WM>(h + 1, h < 16);
    }
    WAIT_BAR(0); step<0, WM>(18, false); step<1, WM>(19, false);
  }
};

template <int MODE>
__device__ __forceinline__ void k1_tile(char* smem, const u16* __restrict__ x, const u16* __restrict__ w0, const u16* __restrict__ w1,
                                        char* __restrict__ ws, int rb, int ct, int tid, int wave) {
  constexpr int NB = K1Core<MODE>::NB, LD = MODE == 2 ? F_ : N1_;
  const int lane = tid & 63, i = lane & 15, g = lane >> 4, wm = wave >> 1, wn = wave & 1;
  K1Core<MODE> c;
  c.wave = wave; c.lds0 = (unsigned)(uintptr_t)smem; c.lb = (lds_cptr)smem; c.wstep = 32 * LD;
  c.xs = x + (rb * 128 + wave * 16) * D_;
  c.xl = (lane >> 3) * D_ * 2 + (((lane & 7) ^ g) << 4);
  {
    constexpr int CPR = K1Core<MODE>::ROW / 16;
    const int pa = MODE == 2 && wave >= 6 ? wave - 6 : wave, pb = MODE == 2 ? 2 + (wave & 3) : 8 + (wave & 1);
    const int ca = pa * 64 + lane, ra = ca / CPR, cb = pb * 64 + lane, rc = cb / CPR;
    c.wla = ra * LD * 2 + (((ca - ra * CPR) ^ ((ra >> 2) & 2)) << 4);
    c.wlb = rc * LD * 2 + (((cb - rc * CPR) ^ ((rc >> 2) & 2)) << 4);
    c.wpa = (wave >= 6 ? w1 : w0) + ct * (CPR * 8);
    c.wpb = w1 + ct * (CPR * 8);
  }
  c.xo = X0 + wave * 2048 + i * 128 + ((g ^ (i >> 1)) << 4);
  {
    const int q = i >> 2, p = lane & 3;
#pragma unroll
    for (int j = 0; j < 5; j++) {
      const int Q = MODE == 0 ? 5 * p + j + 20 * wn : MODE == 1 ? 4 * j + p + 20 * wn : 4 * (wn ? (j == 2 ? 3 : 4 + j) : j) + p;
      c.wo[j] = (8 * g + q) * K1Core<MODE>::ROW + ((Q ^ (4 * (g & 1))) << 3);
    }
    const unsigned v = i == 0 ? 0x3f803f80u : 0u;
    c.sel = __builtin_bit_cast(bf16x8, u32x4{v, v, v, v});
  }
  if (wm == 0) c.template run<0>();
  else if (wm == 1) c.template run<1>();
  else if (wm == 2) c.template run<2>();
  else c.template run<3>();
  float* sc = (float*)smem;
  if (g == 0) {
    sc[wave * 16 + i] = c.rs[0];
    sc[1792 + wn * 96 + wm * 16 + i] = c.cs[0][0];
    if (wm + 4 < NB) sc[1792 + wn * 96 + (wm + 4) * 16 + i] = c.cs[1][0];
  }
  if (MODE != 2 && g == (i >> 2)) {
    const float s01 = (i & 1) ? c.sq[1] : c.sq[0], s23 = (i & 1) ? c.sq[3] : c.sq[2];
    sc[256 + wave * 16 + i] = (i & 2) ? s23 : s01;
  }
  LGKM_BAR();
  if (tid < 128) {
    if (MODE == 2) {
      sc[1536 + tid] = -bfr(sc[tid] * (1.0f / D_));
    } else {
      float mean_b, std_b;
      ln_stats(sc[tid], sc[256 + tid], mean_b, std_b);
      const float r = 1.0f / std_b;
      sc[1280 + tid] = r; sc[1536 + tid] = -mean_b * r;
      if (ct == 0) __hip_atomic_store((gu32*)(ws + WS_ST) + rb * 128 + tid, __float_as_uint(r * r), RLX);
    }
  }
  LGKM_BAR();
  if (MODE == 0) {
    const bool isq = ct < 4;
    const float scl = isq ? 0.22360679774997896f * 1.4426950408889634f : 1.0f;
    const int head = (ct & 3) * 8 + wn * 4 + g;
    f32x4 cv[5];
#pragma unroll
    for (int j = 0; j < 5; j++) cv[j] = *(const f32x4*)(sc + 1792 + wn * 96 + j * 16 + 4 * g);
#pragma unroll
    for (int a = 0; a < 2; a++) {
      const int blk = a ^ wn, row = wm * 32 + blk * 16 + i, odd = wm & 1;
      const float r = sc[1280 + row] * scl, m = sc[1536 + row] * scl;
      unsigned w[10];
#pragma unroll
      for (int j = 0; j < 5; j++) {
        w[2 * j] = pk2(c.acc[j][a][0] * r + m * cv[j][0], c.acc[j][a][1] * r + m * cv[j][1]);
        w[2 * j + 1] = pk2(c.acc[j][a][2] * r + m * cv[j][2], c.acc[j][a][3] * r + m * cv[j][3]);
      }
      const int rr = isq ? blk * 16 + i : 16 * (i >> 3) + 8 * blk + (i & 7);
      char* tb = isq ? ws + WS_Q + (head * 64 + rb * 4 + wm) * QT : ws + WS_KV + head * KVH + (rb * 2 + (wm >> 1)) * KVP + odd * KVT;
      ST16(tb + rr * 16, (u32x4{w[0], w[1], w[2], w[3]}));
      ST16(tb + 512 + rr * 16, (u32x4{w[4], w[5], w[6], w[7]}));
      if (isq) ST8(tb + 1024 + rr * 8, (u32x2{w[8], w[9]}));
      else *(u32x2*)(smem + 8192 + (((wm >> 1) * 2 + wn) * 4 + g) * 512 + rr * 16 + odd * 8) = u32x2{w[8], w[9]};
    }
    if (!isq) {
      LGKM_BAR();
      const int hl = (wm & 1) * 2 + (lane >> 5);
      ST16(ws + WS_KV + ((ct & 3) * 8 + wn * 4 + hl) * KVH + (rb * 2 + (wm >> 1)) * KVP + 2 * KVT + (lane & 31) * 16,
           *(const u32x4*)(smem + 8192 + (((wm >> 1) * 2 + wn) * 4 + hl) * 512 + (lane & 31) * 16));
    }
  } else if (MODE == 1) {
    {
      const int odd = wm & 1, row = wm * 32 + 4 * g;
      const f32x4 r0 = *(const f32x4*)(sc + 1280 + row), r1 = *(const f32x4*)(sc + 1296 + row);
      const f32x4 m0 = *(const f32x4*)(sc + 1536 + row), m1 = *(const f32x4*)(sc + 1552 + row);
#pragma unroll
      for (int j = 0; j < 5; j++) {
        const int n = 16 * j + i, hl = (n * 52429) >> 20, d = n - hl * 20;
        const float cv = sc[1792 + wn * 96 + n];
        const f32x4 u = (wn ? c.acc[j][1] : c.acc[j][0]) * r0 + m0 * cv, v = (wn ? c.acc[j][0] : c.acc[j][1]) * r1 + m1 * cv;
        char* tb = smem + 8192 + wave * 5632 + hl * 1408 + g * VB + d * 16;
        *(u32x4*)tb = u32x4{pk2(u[0], u[1]), pk2(u[2], u[3]), pk2(v[0], v[1]), pk2(v[2], v[3])};
        if (j > 0 && d >= 18) { const unsigned k = (d == 18) ? 0x3f803f80u : 0u; *(u32x4*)(tb + 32) = u32x4{k, k, k, k}; }
      }
      asm volatile("s_waitcnt lgkmcnt(0)" ::: "memory");
      char* vb = ws + WS_KV + ((ct & 3) * 8 + wn * 4) * KVH + (rb * 2 + (wm >> 1)) * KVP + odd * KVT + 1024;
#pragma unroll
      for (int e = 0; e < 6; e++) {
        const int n = e * 64 + lane, hl = n / 88;
        if (n < 352) ST16(vb + hl * (KVH - 1408) + n * 16, *(const u32x4*)(smem + 8192 + wave * 5632 + n * 16));
      }
    }
  } else {
    f32x4 cv[6];
#pragma unroll
    for (int j = 0; j < 6; j++) cv[j] = *(const f32x4*)(sc + 1792 + wn * 96 + j * 16 + 4 * g);
#pragma unroll
    for (int a = 0; a < 2; a++) {
      const int sub = (wm & 1) * 2 + (a ^ wn);
      const float m = sc[1536 + wm * 32 + (a ^ wn) * 16 + i];
      unsigned w[6];
#pragma unroll
      for (int j = 0; j < 3; j++)
#pragma unroll
        for (int e = 0; e < 2; e++) {
          const unsigned gp = pk2(c.acc[j][a][2 * e] + m * cv[j][2 * e], c.acc[j][a][2 * e + 1] + m * cv[j][2 * e + 1]);
          const unsigned up = pk2(c.acc[j + 3][a][2 * e] + m * cv[j + 3][2 * e], c.acc[j + 3][a][2 * e + 1] + m * cv[j + 3][2 * e + 1]);
          w[2 * j + e] = pk2(bflo(gp) * bflo(up), bfhi(gp) * bfhi(up));
        }
      char* ab = ws + WS_A3 + (((rb * 2 + (wm >> 1)) * KS3 + 20 + ct * 3) * 4 + sub) * 1024;
      ST16(ab + wn * 8192 + lane * 16, (u32x4{w[0], w[1], w[2], w[3]}));
      ST8(ab + 4096 + wn * 512 + (g >> 1) * 256 + i * 16 + (g & 1) * 8, (u32x2{w[4], w[5]}));
    }
  }
}

__device__ __forceinline__ void retile_role(char* smem, const u16* __restrict__ wo, const u16* __restrict__ wd, char* __restrict__ ws,
                                            int idx, int tid, int wave) {
  const int lane = tid & 63, g = lane >> 4, q = (lane & 15) >> 2, p = lane & 3;
  const unsigned lds0 = (unsigned)(uintptr_t)smem;
  for (int k0 = idx; k0 < KS3; k0 += 64) {
    WAIT_BAR(0);
    for (int u = 0; u < 2; u++) {
      const int ks = k0 + 32 * u;
      if (ks < KS3) {
        const int orow = ks < 16 ? (2 * ks + (wave >> 2)) * 20 + (wave & 2) * 2 + (wave & 1) * 8 : (8 * (ks - 16) + wave) * 20 + 16;
        const char* src = (const char*)(ks < 20 ? wo + (size_t)orow * D_ : wd + (size_t)((ks - 20) * 32 + wave * 4) * D_) + lane * 16;
#pragma unroll
        for (int e = 0; e < 5; e++) glds16(src + e * 1024, lds0 + u * 40960 + wave * 5120 + e * 1024);
      }
    }
    WAIT_BAR(0);
    for (int u = 0; u < 2; u++) {
      const int ks = k0 + 32 * u;
      if (ks < KS3) {
        const bool nat = ks < 20 || ks % 3 == 0;
        const int row = nat ? 8 * g + q : 4 * g + q, sec = (nat ? 4 : 16) * 1280;
        lds_cptr b = (lds_cptr)smem + u * 40960 + row * 1280 + wave * 160;
#pragma unroll
        for (int t = 0; t < 5; t++)
          ST16(ws + WS_W3 + ((wave * KS3 + ks) * 5 + t) * 1024 + lane * 16, __builtin_bit_cast(u32x4, ldstr2(b + (t < 4 ? (t >> 1) * 64 + p * 16 + (t & 1) * 8 : 128 + p * 8), sec)));
      }
    }
  }
}

constexpr int AT_BUF = 4 * KVP;
constexpr int K2_LDS = 81920;

#define TOFF(t) (((t) >> 1) * KVP + ((t) & 1) * KVT)
struct KFrag { bf16x8 k1; s16x4 k2; };

__device__ __forceinline__ f32x16 qk_tile(const KFrag& k, bf16x8 q1, s16x4 q2) {
  f32x16 z = {};
  f32x16 s = MFMA32(k.k1, q1, z);
  return MFMA32X8(k.k2, q2, s);
}
__device__ __forceinline__ bf16x8 exp_pack(const f32x16& s, int m) {
  u32x4 w;
#pragma unroll
  for (int i = 0; i < 4; i++) w[i] = pk2(__builtin_amdgcn_exp2f(s[8 * m + 2 * i]), __builtin_amdgcn_exp2f(s[8 * m + 2 * i + 1]));
  return __builtin_bit_cast(bf16x8, w);
}
__device__ __forceinline__ float max16(const f32x16& s) {
  float a = fmaxf(s[0], s[1]);
#pragma unroll
  for (int i = 2; i < 16; i += 2) a = fmaxf(fmaxf(a, s[i]), s[i + 1]);
  return a;
}

#define AT_GROUP(MF, ACC, A, B, P, PJ, SJ) do { unsigned pa_, pb_; SBAR();                                                        \
    asm volatile(MF "\n\tv_cvt_pk_bf16_f32 %1, %3, %4\n\tv_cvt_pk_bf16_f32 %2, %5, %6\n\tv_exp_f32 %3, %9\n\tv_exp_f32 %4, %10\n\t"    \
                 "v_exp_f32 %5, %11\n\tv_exp_f32 %6, %12"                                                                          \
                 : ACC, "=&v"(pa_), "=&v"(pb_), "+v"(e[0]), "+v"(e[1]), "+v"(e[2]), "+v"(e[3])                                     \
                 : "v"(A), "v"(B), "v"(sa[SJ]), "v"(sa[SJ + 1]), "v"(sa[SJ + 2]), "v"(sa[SJ + 3]));                                \
    P[PJ] = pa_; P[PJ + 1] = pb_; SBAR(); } while (0)

__device__ __forceinline__ void attn_role(char* smem, char* __restrict__ ws, unsigned tag, int head, int qb, int tid, int wave) {
  const int lane = tid & 63, r = lane & 31, hh = lane >> 5;
  const unsigned lds0 = (unsigned)(uintptr_t)smem;
  const char* kvsrc = ws + WS_KV + (size_t)head * KVH + wave * 1024 + lane * 16;
  auto dma = [&](int c, int buf) {
    const char* src = kvsrc + (size_t)c * AT_BUF;
    const unsigned dst = lds0 + buf + wave * 1024;
    glds16d(src, dst);
    glds16d(src + 8192, dst + 8192);
    if (wave < 5) glds16d(src + 16384, dst + 16384);
  };
  if (wave == 0) poll(ws, qb * FREP + FQ + (head >> 3) * 64 + min(lane, 47), tag);
  LGKM_BAR();
  dma(0, 0);
  const int qo = (int)WS_Q + (head * 64 + qb * 8 + wave) * QT;
  const bf16x8 q1 = LD16(lane * 16, qo);
  u32x2 q2u = LD8(1024 + r * 8, qo);
  if (hh) q2u = u32x2{0u, 0u};
  const int k1o = lane * 16, k2o = hh ? 1024 + 320 : 2 * KVT + r * 16, vo = 1024 + hh * VB + min(r, 21) * 16;
  lds_cptr lb = (lds_cptr)smem;
  LAS int* flag = (LAS int*)(lb + 3 * AT_BUF + 512);
  auto ldk = [&](int t) { KFrag k; k.k1 = lds128(lb + TOFF(t) + k1o); k.k2 = lds64(lb + (t >> 1) * KVP + (t & 1) * 8 + k2o); return k; };
  auto set_ref = [&](float m) {
    const float m0 = bfr(m), m1 = bfr(m - m0), m2 = bfr(m - m0 - m1);
    if (hh) q2u = u32x2{pk2(-m0, -m1), pk2(-m2, 0.f)};
  };
  WAIT_BAR(0);
  dma(1, AT_BUF);
  {
    KFrag k = ldk(0);
    set_ref(xhalf_max(max16(qk_tile(k, q1, __builtin_bit_cast(s16x4, q2u)))));
  }
  f32x16 o;
  for (int attempt = 0;; attempt++) {
    const s16x4 q2 = __builtin_bit_cast(s16x4, q2u);
    o = f32x16{};
    f32x16 sa;
    { KFrag k = ldk(0); sa = qk_tile(k, q1, q2); }
    bf16x8 k1 = lds128(lb + KVT + k1o), v1 = {}, v0;
    s16x8 k2p = __builtin_bit_cast(s16x8, lds128(lb + k2o));
    v0 = lds128(lb + vo);
    u32x4 p0 = {0u, 0u, 0u, 0u}, p1 = {0u, 0u, 0u, 0u};
    float e[4] = {0.f, 0.f, 0.f, 0.f};
    int cur = 0, nxt = AT_BUF;
    asm volatile("s_nop 15\n\ts_nop 7" : "+v"(sa), "+v"(o), "+v"(p1), "+v"(v1));
    for (int c = 0; c < 8; c++) {
      lds_cptr ck1 = lb + cur + k1o, ck2 = lb + cur + k2o, cv = lb + cur + vo;
      lds_cptr nk1 = lb + nxt + k1o, nk2 = lb + nxt + k2o, nv = lb + nxt + vo;
      const int nn = (nxt == 2 * AT_BUF) ? 0 : nxt + AT_BUF;
#pragma unroll
      for (int t = 0; t < 8; t++) {
        if (t == 6) asm volatile("s_waitcnt vmcnt(0)\n\ts_barrier" ::: "memory");
        const int pc = (t + 2) & 7;
        const bool ld = pc < 3 && (pc < 2 ? c < 6 : (c > 0 && c < 7 && wave < 5));
        const char* src = kvsrc + (size_t)(t == 0 ? c + 1 : c + 2) * AT_BUF + pc * 8192;
        const unsigned dst = lds0 + (t == 0 ? nxt : nn) + wave * 1024 + pc * 8192;
        if (ld && wave < 4) glds16d(src, dst);
        f32x16 sb;
        const s16x4 k2 = (t & 1) ? __builtin_shufflevector(k2p, k2p, 0, 1, 2, 3) : __builtin_shufflevector(k2p, k2p, 4, 5, 6, 7);
        AT_GROUP("v_mfma_f32_32x32x16_bf16 %0, %7, %8, 0", "=&v"(sb), k1, q1, p1, 2, 0);
        k1 = lds128((t < 6 ? ck1 : nk1) + TOFF((t + 2) & 7));
        AT_GROUP("v_mfma_f32_32x32x16_bf16 %0, %7, %8, %0", "+v"(o), v1, p1, p0, 0, 4);
        v1 = lds128(cv + TOFF(t) + 2 * VB);
        if (ld && wave >= 4) glds16d(src, dst);
        AT_GROUP("v_mfma_f32_32x32x8_bf16 %0, %7, %8, %0", "+v"(sb), k2, q2, p0, 2, 8);
        if (!(t & 1)) k2p = __builtin_bit_cast(s16x8, lds128((t < 6 ? ck2 : nk2) + TOFF((t + 2) & 7)));
        AT_GROUP("v_mfma_f32_32x32x16_bf16 %0, %7, %8, %0", "+v"(o), v0, p0, p1, 0, 12);
        v0 = lds128((t < 7 ? cv : nv) + TOFF((t + 1) & 7));
        sa = sb;
      }
      cur = nxt; nxt = nn;
    }
    asm volatile("s_nop 7" : "+v"(o), "+v"(e[3]));
    p1[2] = pk2(e[0], e[1]); p1[3] = pk2(e[2], e[3]);
    o = MFMA32(v1, __builtin_bit_cast(bf16x8, p1), o);
    if (attempt == 1) break;
    bool ok = true;
#pragma unroll
    for (int i = 0; i < 12; i++) ok = ok && (fabsf(o[i]) < 1e30f || (hh && i > 8));
    LGKM_BAR();
    if (tid == 0) *flag = 0;
    LGKM_BAR();
    if (!ok) *flag = 1;
    LGKM_BAR();
    if (*flag == 0) break;
    if (hh) q2u = u32x2{0u, 0u};
    const s16x4 qz = __builtin_bit_cast(s16x4, q2u);
    float mx = -INFINITY;
    for (int c = 0; c < 8; c++) {
      LGKM_BAR();
      dma(c, 0);
      WAIT_BAR(0);
      for (int t = 0; t < 8; t++) { KFrag k = ldk(t); mx = fmaxf(mx, max16(qk_tile(k, q1, qz))); }
    }
    set_ref(xhalf_max(mx));
    LGKM_BAR();
    dma(0, 0); dma(1, AT_BUF);
    WAIT_BAR(0);
  }
  auto sw = __builtin_amdgcn_permlane32_swap(__float_as_uint(o[8]), __float_as_uint(o[8]), false, false);
  const float inv = 1.0f / __uint_as_float(sw[1]);
  const int s = qb * 256 + wave * 32 + r;
  char* ab = ws + WS_A3 + ((s >> 6) * KS3 * 4 + ((s >> 4) & 3)) * 1024;
  ST16(ab + (head >> 1) * 4096 + ((head & 1) * 2 + hh) * 256 + (s & 15) * 16,
       (u32x4{pk2(o[0] * inv, o[1] * inv), pk2(o[2] * inv, o[3] * inv), pk2(o[4] * inv, o[5] * inv), pk2(o[6] * inv, o[7] * inv)}));
  if (!hh) ST8(ab + (16 + (head >> 3)) * 4096 + (head & 7) * 128 + (s & 15) * 8, (u32x2{pk2(o[8] * inv, o[9] * inv), pk2(o[10] * inv, o[11] * inv)}));
  publish(ws, FO + qb * 32 + head, tag, tid);
}

__device__ __forceinline__ void down_role(char* smem, const u16* __restrict__ x, char* __restrict__ ws, u16* __restrict__ out, unsigned tag, int rb, int nb, int tid, int wave) {
  const int lane = tid & 63;
  if (wave == 0) poll(ws, nb * FREP + (lane < 18 ? FG + (rb >> 1) * 32 + lane : lane < 32 ? FQ + (rb >> 1) : FR - 32 + lane), tag);
  LGKM_BAR();
  int ap = (int)WS_A3 + (rb * KS3 + 20 + wave) * 4096, wp = (int)WS_W3 + (nb * KS3 + 20 + wave) * 5120;
  bf16x8 a[4], w[5];
#pragma unroll
  for (int t = 0; t < 5; t++) w[t] = LD16(lane * 16 + (t & 3) * 1024, wp + (t >> 2) * 4096);
#pragma unroll
  for (int i = 0; i < 4; i++) a[i] = LD16(lane * 16 + i * 1024, ap);
  f32x4 acc[20] = {};
  const int n = wave < 6 ? 7 : 6;
  for (int it = 1; it <= n; it++) {
    if (it < n) { ap += 8 * 4096; wp += 8 * 5120; }
#pragma unroll
    for (int i = 0; i < 4; i++) {
#pragma unroll
      for (int t = 0; t < 5; t++) acc[t * 4 + i] = MFMA16(w[t], a[i], acc[t * 4 + i]);
      SBAR();
      a[i] = LD16(lane * 16 + i * 1024, ap);
    }
#pragma unroll
    for (int t = 0; t < 5; t++) w[t] = LD16(lane * 16 + (t & 3) * 1024, wp + (t >> 2) * 4096);
  }
  f32x4* red = (f32x4*)smem;
  const int rbk = wave & 3, kg = wave >> 2, g = lane >> 4, row = rb * 64 + rbk * 16 + (lane & 15);
  const size_t xo = (size_t)row * D_ + nb * 80;
  const u32x4 xv = *(const u32x4*)(x + xo + kg * 32 + g * 8);
  const u32x2 xw = *(const u32x2*)(x + xo + 64 + g * 4);
  const float r2 = __uint_as_float(__hip_atomic_load((gu32*)(ws + WS_ST) + row, RLX));
  f32x4 fin[3];
#pragma unroll
  for (int rd = 0; rd < 3; rd++) {
    if (rd) LGKM_BAR();
#pragma unroll
    for (int u = 0; u < (rd < 2 ? 8 : 4); u++) red[(wave * 8 + u) * 64 + lane] = acc[(rd < 2 ? 2 * (u >> 2) + rd : 4) * 4 + (u & 3)];
    LGKM_BAR();
    f32x4 v = red[(rd < 2 ? wave : rbk) * 64 + lane];
#pragma unroll
    for (int k = 1; k < 8; k++) v += red[(k * 8 + (rd < 2 ? wave : rbk)) * 64 + lane];
    const unsigned x0 = rd < 2 ? xv[2 * rd] : xw[0], x1 = rd < 2 ? xv[2 * rd + 1] : xw[1];
    fin[rd] = v * r2 + f32x4{bflo(x0), bfhi(x0), bflo(x1), bfhi(x1)};
    PIN(fin[rd]);
  }
  LGKM_BAR();
  const char* wo = ws + WS_W3 + ((size_t)nb * KS3 + kg * 10) * 5120 + lane * 16;
  const unsigned lds0 = (unsigned)(uintptr_t)smem;
#pragma unroll
  for (int e = 0; e < 10; e++) glds16d(wo + (10 + rbk * 10 + e) * 1024, lds0 + (wave * 10 + e) * 1024);
  bf16x8 wr[10];
#pragma unroll
  for (int e = 0; e < 10; e++) wr[e] = LD16(lane * 16 + (e & 3) * 1024, (int)WS_W3 + (nb * KS3 + kg * 10) * 5120 + (e >> 2) * 4096);
  f32x4 c[5];
  c[0] = kg ? f32x4{} : fin[0]; c[1] = kg ? f32x4{} : fin[1]; c[2] = kg ? fin[0] : f32x4{}; c[3] = kg ? fin[1] : f32x4{}; c[4] = kg ? f32x4{} : fin[2];
#pragma unroll
  for (int e = 0; e < 10; e++) PIN(wr[e]);
  asm volatile("" : "+v"(c[0]), "+v"(c[1]), "+v"(c[2]), "+v"(c[3]), "+v"(c[4]));
  if (rb == 0 && nb == 0) poll(ws, FG + (tid >> 5) * 32 + min(tid & 31, 17), tag);
  if (wave == 0) poll(ws, nb * FREP + FO + (rb >> 2) * 32 + (lane & 31), tag);
  WAIT_BAR(0);
  if (rb == 0 && nb == 0 && tid == 0) __hip_atomic_store((gu32*)(ws + WS_FLG), tag, RLX);
  const int ob = (int)WS_A3 + ((rb * KS3 + kg * 10) * 4 + rbk) * 1024, o2 = kg ? g * 256 + (lane & 15) * 8 : lane * 16, o3 = o2 + (kg ? 128 : 8);
  bf16x8 of[10];
#pragma unroll
  for (int k = 0; k < 10; k++) {
    if (k < 6) of[k] = LD16(lane * 16, ob + k * 4096);
    else {
      const u32x2 lo = LD8(o2, ob + k * 4096), hi = LD8(o3, ob + k * 4096);
      of[k] = __builtin_bit_cast(bf16x8, u32x4{lo[0], lo[1], hi[0], hi[1]});
    }
  }
  SBAR();
  lds_cptr lw = (lds_cptr)smem + kg * 40960 + lane * 16;
#pragma unroll
  for (int k = 0; k < 10; k++) {
#pragma unroll
    for (int t = 0; t < 5; t++) c[t] = MFMA16(wr[(k & 1) * 5 + t], of[k], c[t]);
    asm volatile("" : "+v"(c[0]), "+v"(c[1]), "+v"(c[2]), "+v"(c[3]), "+v"(c[4]) :: "memory");
    if (k < 8)
#pragma unroll
      for (int t = 0; t < 5; t++) wr[(k & 1) * 5 + t] = lds128(lw + (k * 5 + t) * 1024);
    SBAR();
  }
  LGKM_BAR();
  red[(wave * 3) * 64 + lane] = kg ? c[0] : c[2];
  red[(wave * 3 + 1) * 64 + lane] = kg ? c[1] : c[3];
  if (kg) red[(wave * 3 + 2) * 64 + lane] = c[4];
  LGKM_BAR();
  const f32x4* pr = red + ((wave ^ 4) * 3) * 64 + lane;
  const f32x4 v0 = (kg ? c[2] : c[0]) + pr[0], v1 = (kg ? c[3] : c[1]) + pr[64];
  *(u32x4*)(out + xo + kg * 32 + g * 8) = u32x4{pk2(v0[0], v0[1]), pk2(v0[2], v0[3]), pk2(v1[0], v1[1]), pk2(v1[2], v1[3])};
  if (!kg) { const f32x4 v2 = c[4] + pr[128]; *(u32x2*)(out + xo + 64 + g * 4) = u32x2{pk2(v2[0], v2[1]), pk2(v2[2], v2[3])}; }
}

__global__ __launch_bounds__(512, 4) void fused_block(const u16* __restrict__ x, const u16* __restrict__ wqkv, const u16* __restrict__ wg,
                                                      const u16* __restrict__ wu, const u16* __restrict__ wo, const u16* __restrict__ wd,
                                                      char* __restrict__ ws, u16* __restrict__ out) {
  __shared__ __attribute__((aligned(16))) char smem[K2_LDS];
  const int tid = threadIdx.x;
  const int wave = __builtin_amdgcn_readfirstlane(tid >> 6);
  const int b = blockIdx.x, xcd = b & 7, o = (b >> 3) & 31, cg = xcd >> 1, t = o >> 3, rb = (xcd & 1) * 8 + (o & 7);
  int slot;
  if (b >= 256) {
    const int u = min(tid - 320, 159);
    const char* pf = u < 0 ? (const char*)(x + (rb * 128 + t * 32) * D_) + tid * 128 : (const char*)(wqkv + (o + (u >> 3) * 32) * N1_ + cg * 480) + (u & 7) * 128;
    const unsigned dst = (unsigned)(uintptr_t)smem + 3 * STG + wave * 256;
    asm volatile("s_mov_b32 m0, %1\n\ts_nop 0\n\tglobal_load_lds_dword %0, off" :: "v"(pf), "s"(dst) : "memory", "m0");
    __builtin_amdgcn_s_sleep(96);
    if (wave < 5 || (wave < 7 && !(cg & 1))) {
      const int kr = wave < 5 ? (o & 7) + (tid >> 2) * 8 : o + (min(u, 79) >> 2) * 32;
      pf = (const char*)((tid & 2 ? wu : wg) + kr * F_ + ((cg >> 1) * 9 + (wave < 5 ? (cg & 1) * 5 + t : 4)) * 96) + (tid & 1) * 188;
      asm volatile("s_mov_b32 m0, %1\n\ts_nop 0\n\tglobal_load_lds_dword %0, off" :: "v"(pf), "s"(dst) : "memory", "m0");
    }
  }
  if (b < 256) __builtin_amdgcn_s_setprio(2); else __builtin_amdgcn_s_setprio(1);
  if (b < 256 && t < 3) {
    const int ct = cg * 3 + t;
    if (ct < 8) k1_tile<0>(smem, x, wqkv, wqkv, ws, rb, ct, tid, wave);
    else k1_tile<1>(smem, x, wqkv, wqkv, ws, rb, ct, tid, wave);
    slot = FQ + (ct & 3) * 64 + (ct >> 2) * 16 + rb;
  } else if (b >= 256 || !(cg & 1)) {
    const int ct = (cg >> 1) * 9 + (b < 256 ? 4 : (cg & 1) * 5 + t);
    k1_tile<2>(smem, x, wg, wu, ws, rb, ct, tid, wave);
    slot = FG + rb * 32 + ct;
  } else {
    __builtin_amdgcn_s_sleep(80); __builtin_amdgcn_s_sleep(80);
    retile_role(smem, wo, wd, ws, (cg >> 1) * 16 + rb, tid, wave);
    slot = FR + (cg >> 1) * 16 + rb;
  }
  const unsigned tag = __builtin_amdgcn_readfirstlane(__hip_atomic_load((gu32*)(ws + WS_FLG), RLX)) + 1u;
  publish(ws, slot, tag, tid);
  if (b < 256) __builtin_amdgcn_s_setprio(1); else __builtin_amdgcn_s_setprio(0);
  if (b < 256) {
    attn_role(smem, ws, tag, xcd * 4 + (o >> 3), o & 7, tid, wave);
  } else {
    down_role(smem, x, ws, out, tag, (xcd & 3) * 8 + (o & 7), (xcd >> 2) * 4 + (o >> 3), tid, wave);
  }
}

extern "C" void kernel_launch(void* const* d_in, const int* in_sizes, int n_in, void* d_out, int out_size, void* d_ws, size_t ws_size,
                              hipStream_t stream) {
  fused_block<<<512, 512, 0, stream>>>((const u16*)d_in[0], (const u16*)d_in[1], (const u16*)d_in[4], (const u16*)d_in[5], (const u16*)d_in[2],
                                       (const u16*)d_in[6], (char*)d_ws, (u16*)d_out);
}
```
